```python
import torch

def _softmax(x, axis=-1):
    return torch.softmax(x, dim=axis)
def _gelu(x):
    return torch.nn.functional.gelu(x, approximate="tanh")
def _silu(x):
    return torch.nn.functional.silu(x)
def _sigmoid(x):
    return torch.sigmoid(x)
def _layernorm(x, axis=-1, eps=1e-5):
    m = x.mean(dim=axis, keepdim=True)
    v = x.var(dim=axis, keepdim=True, unbiased=False)
    return (x - m) / torch.sqrt(v + eps)
def _scatter_add(tgt, idx, src, axis=0):
    return tgt.clone().index_add_(axis, idx, src)
def _segment_sum(src, seg, n):
    out = torch.zeros((n,) + src.shape[1:], dtype=src.dtype)
    return out.index_add_(0, seg, src)
def _pad(x, pw):
    flat = tuple(v for pair in reversed(pw) for v in pair)
    return torch.nn.functional.pad(x, flat)

kernel_name = 'concat_heads__linear_attn_block__sliding_local_dilated__prompt'

def setup_inputs():

    _w = (lambda a: (lambda t: t.to(torch.bfloat16) if t.is_floating_point() else t)(torch.as_tensor(a)))
    def _A_setup():
        B, S, D = 1, 2048, 448
        return {
            "x": _w((lambda *s: torch.randn(*s, dtype=torch.bfloat16) * 0.05)(B, S, D)),
            "wqkv": _w((lambda *s: torch.randn(*s, dtype=torch.bfloat16) * 1.6)(D, 3 * D)),
            "wo": _w((lambda *s: torch.randn(*s, dtype=torch.bfloat16) * 0.8)(D, D)),
        }
    _A_in = _A_setup()

    def _B_setup():
        B, S, D = 1, 2048, 448
        return {
            "x": _w((lambda *s: torch.randn(*s, dtype=torch.bfloat16) * 0.2)(B, S, D)),
            "wqkv": _w((lambda *s: torch.randn(*s, dtype=torch.bfloat16) * 0.2)(D, 3 * D)),
            "wo": _w((lambda *s: torch.randn(*s, dtype=torch.bfloat16) * 0.05)(D, D)),
        }
    _B_in = _B_setup()

    return {
        "A_x": _A_in["x"],
        "A_wqkv": _A_in["wqkv"],
        "A_wo": _A_in["wo"],
        "B_x": _B_in["x"],
        "B_wqkv": _B_in["wqkv"],
        "B_wo": _B_in["wo"],
    }

def reference(A_x, A_wqkv, A_wo, B_x, B_wqkv, B_wo):
    def _A_ref(x, wqkv, wo):
        B, S, D = x.shape
        H = 16; Dh = D // H
        qkv = (lambda x, *d: x.permute(*d))((x @ wqkv).reshape(B, S, 3, H, Dh), 2, 0, 3, 1, 4)
        q, k, v = qkv[0], qkv[1], qkv[2]
        phi = lambda t: torch.where(t > 0, t + 1, torch.exp(t)) + 1e-6
        pq, pk = phi(q), phi(k)
        kv = torch.einsum("bhsd,bhse->bhde", pk, v)
        z = torch.einsum("bhsd->bhd", pk)
        num = torch.einsum("bhqd,bhde->bhqe", pq, kv)
        den = torch.einsum("bhqd,bhd->bhq", pq, z)[..., None] + 1e-6
        o = num / den
        return (lambda x, *d: x.permute(*d))(o, 0, 2, 1, 3).reshape(B, S, D) @ wo

    def _B_ref(x, wqkv, wo):
        B, S, D = x.shape
        H = 16; Dh = D // H
        W, dil = 8, 2
        qkv = (lambda x, *d: x.permute(*d))((x @ wqkv).reshape(B, S, 3, H, Dh), 2, 0, 3, 1, 4)
        q, k, v = qkv[0], qkv[1], qkv[2]
        scale = Dh ** -0.5
        pos = torch.arange(S)
        off = torch.arange(W) * dil
        idx = pos[:, None] - off[None, :]
        mask = idx >= 0
        idx_c = torch.where(mask, idx, 0).reshape(-1)
        kg = k[:, :, idx_c, :].reshape(B, H, S, W, Dh)
        vg = v[:, :, idx_c, :].reshape(B, H, S, W, Dh)
        logits = torch.einsum("bhsd,bhswd->bhsw", q, kg) * scale
        logits = torch.where(mask[None, None, :, :], logits, -1e9)
        attn = _softmax(logits, axis=-1)
        o = torch.einsum("bhsw,bhswd->bhsd", attn, vg)
        return (lambda x, *d: x.permute(*d))(o, 0, 2, 1, 3).reshape(B, S, D) @ wo

    a = _A_ref(A_x, A_wqkv, A_wo)
    b = _B_ref(B_x, B_wqkv, B_wo)
    return torch.concatenate([a, b], axis=-1)
```

```cpp
#include <hip/hip_runtime.h>
#include <hip/hip_bf16.h>
#include <stdint.h>

#define DI __device__ __forceinline__
#define LAS __attribute__((address_space(3)))
#define GAS __attribute__((address_space(1)))

typedef __attribute__((ext_vector_type(8))) __bf16 bf16x8;
typedef __attribute__((ext_vector_type(2))) __bf16 bf16x2;
typedef __attribute__((ext_vector_type(2))) float f32x2;
typedef __attribute__((ext_vector_type(4))) float f32x4;
typedef __attribute__((ext_vector_type(4))) unsigned u32x4;
typedef __attribute__((ext_vector_type(2))) unsigned u32x2;
typedef __attribute__((ext_vector_type(4))) short s16x4;
typedef unsigned long long u64;
typedef GAS u64 gu64;
typedef __amdgpu_buffer_rsrc_t rsrc_t;

namespace {
constexpr int XROW = 896;
constexpr int WROW = 2688;
constexpr int IMG = 50176;
constexpr int L_Q = 0, L_K = IMG, L_V = 2 * IMG;
constexpr int L_WO = 62592;
constexpr int LDS_BYTES = 163840;
constexpr unsigned WS_FLAG = 4096;
constexpr int F_KV = 0, F_PQ = 128, F_OB = 256, F_RED = 384;
constexpr unsigned WS_PART = 1u << 20;
constexpr unsigned WS_KVF = 2u << 20;
constexpr unsigned WS_PQ = 4u << 20;
constexpr unsigned WS_OB = 6u << 20;
constexpr unsigned WS_BYTES = 8u << 20;
constexpr unsigned SPIN_CAP = 1u << 20;
}

DI unsigned pk2(float a, float b) { f32x2 p = {a, b}; return __builtin_bit_cast(unsigned, __builtin_convertvector(p, bf16x2)); }
DI float bfr(float a) { return (float)(__bf16)a; }
DI float lo16(unsigned u) { return __builtin_bit_cast(float, u << 16); }
DI float hi16(unsigned u) { return __builtin_bit_cast(float, u & 0xffff0000u); }
DI unsigned phi2(float a, float b) {
  unsigned u = pk2(a, b);
  const float t0 = lo16(u), t1 = hi16(u);
  const float e0 = __builtin_amdgcn_exp2f(t0 * 1.4426950408889634f), e1 = __builtin_amdgcn_exp2f(t1 * 1.4426950408889634f);
  u = pk2(t0 > 0.f ? t0 + 1.f : e0, t1 > 0.f ? t1 + 1.f : e1);
  return pk2(lo16(u) + 1e-6f, hi16(u) + 1e-6f);
}
DI unsigned rmul2(float a, float b, float s) { const unsigned u = pk2(a, b); return pk2(lo16(u) * s, hi16(u) * s); }
DI f32x4 mm(u32x4 a, u32x4 b, f32x4 c) {
  return __builtin_amdgcn_mfma_f32_16x16x32_bf16(__builtin_bit_cast(bf16x8, a), __builtin_bit_cast(bf16x8, b), c, 0, 0, 0);
}
DI u32x4 trf(const LAS char* p0, const LAS char* p1) {
  const u32x2 a = __builtin_bit_cast(u32x2, __builtin_amdgcn_ds_read_tr16_b64_v4i16((LAS s16x4*)p0));
  const u32x2 b = __builtin_bit_cast(u32x2, __builtin_amdgcn_ds_read_tr16_b64_v4i16((LAS s16x4*)p1));
  u32x4 r; r[0] = a[0]; r[1] = a[1]; r[2] = b[0]; r[3] = b[1]; return r;
}
DI void glds(unsigned m0v, unsigned voff, rsrc_t rs, unsigned soff) {
  asm volatile("s_mov_b32 m0, %0\n\ts_nop 0\n\tbuffer_load_dwordx4 %1, %2, %3 offen lds"
               :: "s"(m0v), "v"(voff), "s"(rs), "s"(soff) : "m0", "memory");
}
#define GLD16(dst, ptr, off) asm volatile("global_load_dwordx4 %0, %1, off offset:%2" : "=v"(dst) : "v"(ptr), "i"(off) : "memory")
#define VMWAIT(n) asm volatile("s_waitcnt vmcnt(%0)" :: "i"(n) : "memory")
#define LGWAIT() asm volatile("s_waitcnt lgkmcnt(0)" ::: "memory")
#define TIE(v) asm volatile("" : "+v"(v))
DI void bar() { asm volatile("" ::: "memory"); __builtin_amdgcn_s_barrier(); asm volatile("" ::: "memory"); }
DI void st_sc1(u32x4 v, rsrc_t rs, unsigned off) { __builtin_amdgcn_raw_buffer_store_b128(v, rs, off, 0, 16); }
DI u32x4 ld_sc1(rsrc_t rs, unsigned off) { return __builtin_amdgcn_raw_buffer_load_b128(rs, off, 0, 16); }
DI void set_flag(char* ws, int idx, u64 tag) {
  __hip_atomic_store((gu64*)(ws + WS_FLAG + idx * 128), tag, __ATOMIC_RELAXED, __HIP_MEMORY_SCOPE_AGENT);
}
DI void wait_flags(char* ws, int idx, u64 tag) {
  const int ii = idx < 0 ? 0 : idx;
  for (unsigned spins = 0; spins < SPIN_CAP; ++spins) {
    const u64 v = __hip_atomic_load((gu64*)(ws + WS_FLAG + ii * 128), __ATOMIC_RELAXED, __HIP_MEMORY_SCOPE_AGENT);
    if (__all(idx < 0 || v == tag)) break;
    __builtin_amdgcn_s_sleep(1);
  }
  __builtin_amdgcn_fence(__ATOMIC_ACQUIRE, "wavefront");
}

DI void wo_prefetch(const char* wo, LAS char* lds, unsigned ldsb, int cg, int w, int lane) {
  const rsrc_t rs = __builtin_amdgcn_make_buffer_rsrc((void*)wo, 0, 448 * XROW, 0x00020000);
  if (lane < 56) {
    const unsigned row = lane / 14, seg = lane - row * 14;
    const unsigned voff = row * XROW + seg * 16;
#pragma unroll
    for (int j = 0; j < 14; ++j) {
      const unsigned n = w + 8 * j;
      glds(ldsb + L_WO + n * 896, voff, rs, n * 4 * XROW + cg * 224);
    }
    if (w == 0) { u32x4 z = {0, 0, 0, 0}; *(LAS u32x4*)(lds + L_WO + 448 * 224 + lane * 16) = z; }
  }
}

template <bool IS_A>
DI void phase1(const char* x, const char* W, const char* wo, char* ws, rsrc_t wsr, LAS char* lds, unsigned ldsb,
               int chunk, int hp, int cg, int w, int lane, u64& tag, bool fused) {
  const int mg = w & 3, hh = w >> 2, g = lane >> 4, i = lane & 15;
  const int r0 = chunk * 128;
  const int head = 2 * hp + hh;
  const char* xp0 = x + (size_t)(r0 + 32 * mg + i) * XROW + g * 16;
  const char* xp1 = xp0 + 16 * XROW;
  const rsrc_t wr = __builtin_amdgcn_make_buffer_rsrc((void*)W, 0, 448 * WROW, 0x00020000);
  const unsigned wvo = (unsigned)(((lane & 7) ^ ((w & 1) << 2)) * WROW + (lane >> 3) * 16);
  const unsigned wso = (unsigned)(8 * w * WROW + hp * 112);
  const unsigned wld = ldsb + w * 896;

  u32x4 xf[14][2];
#pragma unroll
  for (int s = 0; s < 7; ++s) {
    if (lane < 56) {
      glds(wld + L_K + s * 7168, wvo, wr, wso + s * 64 * WROW + 896);
      glds(wld + L_V + s * 7168, wvo, wr, wso + s * 64 * WROW + 1792);
      if constexpr (!IS_A) glds(wld + L_Q + s * 7168, wvo, wr, wso + s * 64 * WROW);
    }
    GLD16(xf[2 * s][0], xp0, (2 * s) * 64);
    GLD16(xf[2 * s][1], xp1, (2 * s) * 64);
    GLD16(xf[2 * s + 1][0], xp0, (2 * s + 1) * 64);
    GLD16(xf[2 * s + 1][1], xp1, (2 * s + 1) * 64);
  }
  if constexpr (IS_A) {
    if (lane < 56) {
#pragma unroll
      for (int s = 0; s < 7; ++s) glds(wld + L_Q + s * 7168, wvo, wr, wso + s * 64 * WROW);
    }
  }
  const int q = (lane >> 2) & 3, p = lane & 3;
  int toff[2][2];
#pragma unroll
  for (int t = 0; t < 2; ++t) {
    const int cc = 28 * hh + 16 * t + 4 * p;
#pragma unroll
    for (int rd = 0; rd < 2; ++rd)
      toff[t][rd] = g * 896 + (cc >> 3) * 128 + (((q + 4 * rd) ^ ((g & 1) << 2)) * 16) + ((cc >> 2) & 1) * 8;
  }
  const LAS char* pK[2][2]; const LAS char* pV[2][2]; const LAS char* pQ[2][2];
#pragma unroll
  for (int t = 0; t < 2; ++t)
#pragma unroll
    for (int rd = 0; rd < 2; ++rd) { pK[t][rd] = lds + L_K + toff[t][rd]; pV[t][rd] = lds + L_V + toff[t][rd]; pQ[t][rd] = lds + L_Q + toff[t][rd]; }

  const f32x4 z4 = {0.f, 0.f, 0.f, 0.f};
  if constexpr (IS_A) {
    f32x4 ak[2][2] = {{z4, z4}, {z4, z4}}, av[2][2] = {{z4, z4}, {z4, z4}};
    u32x4 wk[2][2], wv[2][2];
    VMWAIT(49 - 6); TIE(xf[0][0]); TIE(xf[0][1]); TIE(xf[1][0]); TIE(xf[1][1]); bar();
#pragma unroll
    for (int t = 0; t < 2; ++t) { wk[0][t] = trf(pK[t][0], pK[t][1]); wv[0][t] = trf(pV[t][0], pV[t][1]); }
#pragma unroll
    for (int ks = 0; ks < 14; ++ks) {
      if (ks + 1 < 14) {
        if (((ks + 1) & 1) == 0) {
          VMWAIT(49 - 6 * ((ks + 1) / 2 + 1));
          TIE(xf[ks + 1][0]); TIE(xf[ks + 1][1]); TIE(xf[ks + 2][0]); TIE(xf[ks + 2][1]); bar();
        }
#pragma unroll
        for (int t = 0; t < 2; ++t) {
          wk[(ks + 1) & 1][t] = trf(pK[t][0] + (ks + 1) * 3584, pK[t][1] + (ks + 1) * 3584);
          wv[(ks + 1) & 1][t] = trf(pV[t][0] + (ks + 1) * 3584, pV[t][1] + (ks + 1) * 3584);
        }
      }
#pragma unroll
      for (int t = 0; t < 2; ++t)
#pragma unroll
        for (int m = 0; m < 2; ++m) {
          ak[t][m] = mm(xf[ks][m], wk[ks & 1][t], ak[t][m]);
          av[t][m] = mm(xf[ks][m], wv[ks & 1][t], av[t][m]);
        }
    }
    u32x4 pkf[2], vf[2];
#pragma unroll
    for (int t = 0; t < 2; ++t) {
      const unsigned keep = ((t == 1) && (i >= 12)) ? 0u : ~0u;
      const unsigned one = ((t == 1) && (i == 12)) ? 0x3f803f80u : 0u;
#pragma unroll
      for (int m = 0; m < 2; ++m) {
        pkf[t][2 * m] = phi2(ak[t][m][0], ak[t][m][1]) & keep; pkf[t][2 * m + 1] = phi2(ak[t][m][2], ak[t][m][3]) & keep;
        vf[t][2 * m] = (pk2(av[t][m][0], av[t][m][1]) & keep) | one; vf[t][2 * m + 1] = (pk2(av[t][m][2], av[t][m][3]) & keep) | one;
      }
    }
    f32x4 kvp[2][2];
#pragma unroll
    for (int u = 0; u < 2; ++u)
#pragma unroll
      for (int t = 0; t < 2; ++t) kvp[u][t] = mm(pkf[t], vf[u], z4);
    bar();
#pragma unroll
    for (int u = 0; u < 2; ++u)
#pragma unroll
      for (int t = 0; t < 2; ++t) *(LAS f32x4*)(lds + L_K + (w * 4 + u * 2 + t) * 1024 + lane * 16) = kvp[u][t];
    LGWAIT(); bar();
    {
      f32x4 s = *(const LAS f32x4*)(lds + L_K + ((hh * 4 + 0) * 4 + mg) * 1024 + lane * 16);
#pragma unroll
      for (int j = 1; j < 4; ++j) s += *(const LAS f32x4*)(lds + L_K + ((hh * 4 + j) * 4 + mg) * 1024 + lane * 16);
      st_sc1(__builtin_bit_cast(u32x4, s), wsr, WS_PART + (unsigned)(((chunk * 16 + head) * 4 + mg) * 1024 + lane * 16));
    }
    VMWAIT(0); LGWAIT(); TIE(tag); tag += 1; bar();
    if (w == 0 && lane == 0) set_flag(ws, F_KV + chunk * 8 + hp, tag);
    if (fused) wo_prefetch(wo, lds, ldsb, cg, w, lane);
    f32x4 aq[2][2] = {{z4, z4}, {z4, z4}};
    u32x4 wq[2][2];
#pragma unroll
    for (int t = 0; t < 2; ++t) wq[0][t] = trf(pQ[t][0], pQ[t][1]);
#pragma unroll
    for (int ks = 0; ks < 14; ++ks) {
      if (ks + 1 < 14) {
#pragma unroll
        for (int t = 0; t < 2; ++t) wq[(ks + 1) & 1][t] = trf(pQ[t][0] + (ks + 1) * 3584, pQ[t][1] + (ks + 1) * 3584);
      }
#pragma unroll
      for (int t = 0; t < 2; ++t)
#pragma unroll
        for (int m = 0; m < 2; ++m) aq[t][m] = mm(wq[ks & 1][t], xf[ks][m], aq[t][m]);
    }
#pragma unroll
    for (int m = 0; m < 2; ++m) {
      u32x4 o;
#pragma unroll
      for (int t = 0; t < 2; ++t) {
        const unsigned keep = ((t == 1) && (g == 3)) ? 0u : ~0u;
        o[2 * t] = phi2(aq[t][m][0], aq[t][m][1]) & keep; o[2 * t + 1] = phi2(aq[t][m][2], aq[t][m][3]) & keep;
      }
      st_sc1(o, wsr, WS_PQ + (unsigned)(((chunk * 8 + 2 * mg + m) * 16 + head) * 1024 + lane * 16));
    }
    VMWAIT(0); LGWAIT(); bar();
    if (w == 0 && lane == 0) set_flag(ws, F_PQ + chunk * 8 + hp, tag);
  } else {
    f32x4 aq[2][2] = {{z4, z4}, {z4, z4}}, ak[2][2] = {{z4, z4}, {z4, z4}}, av[2][2] = {{z4, z4}, {z4, z4}};
    u32x4 wq[2][2], wk[2][2], wv[2][2];
    u32x4 hx[14];
    const int hrow = (r0 - 16 + i) < 0 ? 0 : (r0 - 16 + i);
    const char* xph = x + (size_t)hrow * XROW + g * 16;
    VMWAIT(49 - 7); TIE(xf[0][0]); TIE(xf[0][1]); TIE(xf[1][0]); TIE(xf[1][1]); bar();
#pragma unroll
    for (int t = 0; t < 2; ++t) { wq[0][t] = trf(pQ[t][0], pQ[t][1]); wk[0][t] = trf(pK[t][0], pK[t][1]); wv[0][t] = trf(pV[t][0], pV[t][1]); }
#pragma unroll
    for (int ks = 0; ks < 14; ++ks) {
      if (ks + 1 < 14) {
        if (((ks + 1) & 1) == 0) {
          const int st = (ks + 1) / 2;
          if (st == 4) {
#pragma unroll
            for (int j = 0; j < 14; ++j) GLD16(hx[j], xph, j * 64);
          }
          VMWAIT(49 - 7 * (st + 1) + (st >= 4 ? 14 : 0));
          TIE(xf[ks + 1][0]); TIE(xf[ks + 1][1]); TIE(xf[ks + 2][0]); TIE(xf[ks + 2][1]); bar();
        }
#pragma unroll
        for (int t = 0; t < 2; ++t) {
          wq[(ks + 1) & 1][t] = trf(pQ[t][0] + (ks + 1) * 3584, pQ[t][1] + (ks + 1) * 3584);
          wk[(ks + 1) & 1][t] = trf(pK[t][0] + (ks + 1) * 3584, pK[t][1] + (ks + 1) * 3584);
          wv[(ks + 1) & 1][t] = trf(pV[t][0] + (ks + 1) * 3584, pV[t][1] + (ks + 1) * 3584);
        }
      }
#pragma unroll
      for (int t = 0; t < 2; ++t)
#pragma unroll
        for (int m = 0; m < 2; ++m) {
          aq[t][m] = mm(wq[ks & 1][t], xf[ks][m], aq[t][m]);
          ak[t][m] = mm(wk[ks & 1][t], xf[ks][m], ak[t][m]);
          av[t][m] = mm(xf[ks][m], wv[ks & 1][t], av[t][m]);
        }
    }
    f32x4 ah = z4;
    {
      VMWAIT(0);
#pragma unroll
      for (int j = 0; j < 14; ++j) TIE(hx[j]);
      const int t = mg & 1;
      const LAS char* h0 = lds + ((mg & 2) ? L_V : L_K) + toff[0][0] + (t ? toff[1][0] - toff[0][0] : 0);
      const LAS char* h1 = lds + ((mg & 2) ? L_V : L_K) + toff[0][1] + (t ? toff[1][1] - toff[0][1] : 0);
      if (mg & 2) {
#pragma unroll
        for (int ks = 0; ks < 14; ++ks) ah = mm(hx[ks], trf(h0 + ks * 3584, h1 + ks * 3584), ah);
      } else {
#pragma unroll
        for (int ks = 0; ks < 14; ++ks) ah = mm(trf(h0 + ks * 3584, h1 + ks * 3584), hx[ks], ah);
      }
    }
    u32x4 qf[2], kf[2];
    u32x2 vfr[2][2];
#pragma unroll
    for (int m = 0; m < 2; ++m) {
#pragma unroll
      for (int t = 0; t < 2; ++t) {
        const unsigned keep = ((t == 1) && (g == 3)) ? 0u : ~0u;
        qf[m][2 * t] = pk2(aq[t][m][0], aq[t][m][1]) & keep; qf[m][2 * t + 1] = pk2(aq[t][m][2], aq[t][m][3]) & keep;
        kf[m][2 * t] = pk2(ak[t][m][0], ak[t][m][1]) & keep; kf[m][2 * t + 1] = pk2(ak[t][m][2], ak[t][m][3]) & keep;
        vfr[t][m][0] = pk2(av[t][m][0], av[t][m][1]); vfr[t][m][1] = pk2(av[t][m][2], av[t][m][3]);
      }
    }
    bar();
    if (fused) wo_prefetch(wo, lds, ldsb, cg, w, lane);
    {
      if (mg < 3) {
        LAS char* e = lds + (hh * 4 + mg + 1) * 2048;
        *(LAS u32x4*)(e + lane * 16) = kf[1];
        *(LAS u32x2*)(e + 1024 + lane * 8) = vfr[0][1];
        *(LAS u32x2*)(e + 1536 + lane * 8) = vfr[1][1];
      }
      LAS char* e0 = lds + (hh * 4) * 2048;
      const unsigned keep = ((chunk == 0) || ((mg == 1) && (g == 3))) ? 0u : ~0u;
      u32x2 hv; hv[0] = pk2(ah[0], ah[1]) & keep; hv[1] = pk2(ah[2], ah[3]) & keep;
      if (mg & 2) *(LAS u32x2*)(e0 + 1024 + (mg & 1) * 512 + lane * 8) = hv;
      else        *(LAS u32x2*)(e0 + lane * 16 + (mg & 1) * 8) = hv;
    }
    LGWAIT(); bar();
    u32x4 kprev; u32x2 vprev[2];
    {
      const LAS char* e = lds + (hh * 4 + mg) * 2048;
      kprev = *(const LAS u32x4*)(e + lane * 16);
      vprev[0] = *(const LAS u32x2*)(e + 1024 + lane * 8);
      vprev[1] = *(const LAS u32x2*)(e + 1536 + lane * 8);
    }
    const float scale = 0.18898223650461362f;
#pragma unroll
    for (int m = 0; m < 2; ++m) {
      const u32x4 kp = m ? kf[0] : kprev;
      const f32x4 sp = mm(kp, qf[m], z4), so = mm(kf[m], qf[m], z4);
      const bool first = (chunk == 0) && (mg == 0) && (m == 0);
      float l[8]; bool ok[8]; float mx = -3.0e38f;
#pragma unroll
      for (int r = 0; r < 4; ++r) {
        const int dp = i + 16 - (4 * g + r), dn = i - (4 * g + r);
        ok[r] = (dp <= 14) && !(dp & 1) && !first;
        ok[4 + r] = (dn >= 0) && (dn <= 14) && !(dn & 1);
        l[r] = bfr(bfr(sp[r]) * scale); l[4 + r] = bfr(bfr(so[r]) * scale);
        mx = fmaxf(mx, ok[r] ? l[r] : -3.0e38f); mx = fmaxf(mx, ok[4 + r] ? l[4 + r] : -3.0e38f);
      }
      mx = fmaxf(mx, __shfl_xor(mx, 16)); mx = fmaxf(mx, __shfl_xor(mx, 32));
      float sum = 0.f;
#pragma unroll
      for (int r = 0; r < 8; ++r) { l[r] = ok[r] ? __builtin_amdgcn_exp2f((l[r] - mx) * 1.4426950408889634f) : 0.f; sum += l[r]; }
      sum += __shfl_xor(sum, 16); sum += __shfl_xor(sum, 32);
      const float inv = 1.f / sum;
      u32x4 pf;
#pragma unroll
      for (int r = 0; r < 4; ++r) pf[r] = pk2(l[2 * r] * inv, l[2 * r + 1] * inv);
      u32x4 o;
#pragma unroll
      for (int u = 0; u < 2; ++u) {
        const u32x2 va = m ? vfr[u][0] : vprev[u], vb = vfr[u][m];
        u32x4 vv; vv[0] = va[0]; vv[1] = va[1]; vv[2] = vb[0]; vv[3] = vb[1];
        const f32x4 oo = mm(vv, pf, z4);
        const unsigned keep = ((u == 1) && (g == 3)) ? 0u : ~0u;
        o[2 * u] = pk2(oo[0], oo[1]) & keep; o[2 * u + 1] = pk2(oo[2], oo[3]) & keep;
      }
      st_sc1(o, wsr, WS_OB + (unsigned)(((chunk * 8 + 2 * mg + m) * 16 + head) * 1024 + lane * 16));
    }
    VMWAIT(0); LGWAIT(); TIE(tag); tag += 1; bar();
    if (w == 0 && lane == 0) set_flag(ws, F_OB + chunk * 8 + hp, tag);
  }
}

DI void reduce_job(char* ws, rsrc_t wsr, int job, int lane, u64 tag, bool fused) {
  const int h = job >> 1, u = job & 1;
  if (fused) wait_flags(ws, lane < 16 ? F_KV + lane * 8 + (h >> 1) : -1, tag);
  u32x4 v[16][2];
#pragma unroll
  for (int c = 0; c < 16; ++c)
#pragma unroll
    for (int t = 0; t < 2; ++t) v[c][t] = ld_sc1(wsr, WS_PART + (unsigned)(((c * 16 + h) * 4 + u * 2 + t) * 1024 + lane * 16));
  f32x4 s0 = __builtin_bit_cast(f32x4, v[0][0]), s1 = __builtin_bit_cast(f32x4, v[0][1]);
#pragma unroll
  for (int c = 1; c < 16; ++c) { s0 += __builtin_bit_cast(f32x4, v[c][0]); s1 += __builtin_bit_cast(f32x4, v[c][1]); }
  u32x4 o; o[0] = pk2(s0[0], s0[1]); o[1] = pk2(s0[2], s0[3]); o[2] = pk2(s1[0], s1[1]); o[3] = pk2(s1[2], s1[3]);
  st_sc1(o, wsr, WS_KVF + (unsigned)(job * 1024 + lane * 16));
  VMWAIT(0);
  if (lane == 0) set_flag(ws, F_RED + job, tag);
}

template <bool IS_A, int NT>
DI void phase2_body(char* out, rsrc_t wsr, LAS char* lds, int tile, int cg, int nt0, int lane) {
  const int g = lane >> 4, i = lane & 15, q = (lane >> 2) & 3, p = lane & 3;
  const f32x4 z4 = {0.f, 0.f, 0.f, 0.f};
  u32x4 of[16];
#pragma unroll
  for (int h = 0; h < 16; ++h) of[h] = ld_sc1(wsr, (IS_A ? WS_PQ : WS_OB) + (unsigned)((tile * 16 + h) * 1024 + lane * 16));
  if constexpr (IS_A) {
#pragma unroll
    for (int hb = 0; hb < 16; hb += 8) {
      u32x4 kv[8][2];
#pragma unroll
      for (int h = 0; h < 8; ++h)
#pragma unroll
        for (int u = 0; u < 2; ++u) kv[h][u] = ld_sc1(wsr, WS_KVF + (unsigned)(((hb + h) * 2 + u) * 1024 + lane * 16));
      f32x4 n0[8], n1[8];
      float den[8];
#pragma unroll
      for (int h = 0; h < 8; ++h) { n0[h] = mm(kv[h][0], of[hb + h], z4); n1[h] = mm(kv[h][1], of[hb + h], z4); }
#pragma unroll
      for (int h = 0; h < 8; ++h) den[h] = __shfl(n1[h][0], 48 + i);
      const unsigned keep = (g == 3) ? 0u : ~0u;
#pragma unroll
      for (int h = 0; h < 8; ++h) {
        const float inv = __builtin_amdgcn_rcpf(bfr(bfr(den[h]) + 1e-6f));
        u32x4 o;
        o[0] = rmul2(n0[h][0], n0[h][1], inv); o[1] = rmul2(n0[h][2], n0[h][3], inv);
        o[2] = rmul2(n1[h][0], n1[h][1], inv) & keep; o[3] = rmul2(n1[h][2], n1[h][3], inv) & keep;
        of[hb + h] = o;
      }
      asm volatile("" ::: "memory");
      __builtin_amdgcn_sched_barrier(0);
    }
  }
  const LAS char* b0 = lds + L_WO + (4 * g + q) * 224 + 8 * p + nt0 * 32;
  f32x4 acc[NT];
#pragma unroll
  for (int n = 0; n < NT; ++n) acc[n] = z4;
#pragma unroll
  for (int h = 0; h < 16; ++h) {
#pragma unroll
    for (int n = 0; n < NT; ++n)
      acc[n] = mm(trf(b0 + h * 28 * 224 + n * 32, b0 + (h * 28 + 16) * 224 + n * 32), of[h], acc[n]);
    if ((h & 3) == 3) __builtin_amdgcn_sched_barrier(0);
  }
  char* op = out + (size_t)(tile * 16 + i) * 1792 + (IS_A ? 0 : 896) + cg * 224 + nt0 * 32 + g * 8;
#pragma unroll
  for (int n = 0; n < NT; ++n) {
    u32x2 o; o[0] = pk2(acc[n][0], acc[n][1]); o[1] = pk2(acc[n][2], acc[n][3]);
    *(u32x2*)(op + n * 32) = o;
  }
}

template <bool IS_A>
DI void phase2(char* out, char* ws, rsrc_t wsr, LAS char* lds, int tt, int cg, int w, int lane, u64 tag, bool fused, bool updater) {
  if (fused) {
    if (w == 0) {
      const int chunk = tt >> 1;
      int idx = -1;
      if (lane < 8) idx = (IS_A ? F_PQ : F_OB) + chunk * 8 + lane;
      else if (IS_A && lane < 40) idx = F_RED + lane - 8;
      wait_flags(ws, idx, tag);
      if (updater) {
        wait_flags(ws, F_OB + lane, tag);
        wait_flags(ws, F_OB + 64 + lane, tag);
        if (lane == 0) __hip_atomic_store((gu64*)ws, tag, __ATOMIC_RELAXED, __HIP_MEMORY_SCOPE_AGENT);
      }
    }
    bar();
  }
  const int tile = tt * 4 + (w & 3);
  if (w < 4) phase2_body<IS_A, 4>(out, wsr, lds, tile, cg, 0, lane);
  else       phase2_body<IS_A, 3>(out, wsr, lds, tile, cg, 4, lane);
}

__global__ void __launch_bounds__(512, 2)
fused_kernel(const char* xA, const char* wA, const char* woA, const char* xB, const char* wB, const char* woB,
             char* out, char* ws, int mode) {
  __shared__ __attribute__((aligned(16))) char smem[LDS_BYTES];
  LAS char* lds = (LAS char*)smem;
  const unsigned ldsb = (unsigned)(uintptr_t)lds;
  const int lane = threadIdx.x & 63;
  const int w = __builtin_amdgcn_readfirstlane(threadIdx.x >> 6);
  const int b = blockIdx.x, xcd = b & 7, idx = b >> 3, xq = xcd >> 1;
  const bool isA = (xcd & 1) == 0;
  const int chunk = (xq & 1) * 8 + (idx & 7), hp = (xq >> 1) * 4 + (idx >> 3);
  const int tt = xq * 8 + (idx & 7), cg = idx >> 3;
  const bool fused = mode == 0;
  const rsrc_t wsr = __builtin_amdgcn_make_buffer_rsrc((void*)ws, 0, WS_BYTES, 0x00020000);
  u64 tag;
  asm volatile("global_load_dwordx2 %0, %1, off sc1" : "=v"(tag) : "v"(ws) : "memory");

  if (mode >= 2) { VMWAIT(0); TIE(tag); }
  if (mode <= 1) {
    if (isA) phase1<true>(xA, wA, woA, ws, wsr, lds, ldsb, chunk, hp, cg, w, lane, tag, fused);
    else     phase1<false>(xB, wB, woB, ws, wsr, lds, ldsb, chunk, hp, cg, w, lane, tag, fused);
  }
  if (mode == 0 || mode == 2) {
    const int ua = xq * 32 + idx;
    if (isA && (ua & 3) == 0 && w == 0) reduce_job(ws, wsr, ua >> 2, lane, tag, fused);
  }
  if (mode == 3) {
    wo_prefetch(isA ? woA : woB, lds, ldsb, cg, w, lane);
    VMWAIT(0); LGWAIT(); bar();
  }
  if (mode == 0 || mode == 3) {
    if (isA) phase2<true>(out, ws, wsr, lds, tt, cg, w, lane, tag, fused, b == 0);
    else     phase2<false>(out, ws, wsr, lds, tt, cg, w, lane, tag, fused, false);
  }
}

extern "C" void kernel_launch(void* const* d_in, const int* in_sizes, int n_in,
                              void* d_out, int out_size, void* d_ws, size_t ws_size,
                              hipStream_t stream) {
  const char* xA = (const char*)d_in[0]; const char* wA = (const char*)d_in[1]; const char* woA = (const char*)d_in[2];
  const char* xB = (const char*)d_in[3]; const char* wB = (const char*)d_in[4]; const char* woB = (const char*)d_in[5];
  fused_kernel<<<256, 512, 0, stream>>>(xA, wA, woA, xB, wB, woB, (char*)d_out, (char*)d_ws, 0);
}
```

```cpp
#include <hip/hip_runtime.h>
#include <hip/hip_bf16.h>
#include <stdint.h>

#define DI __device__ __forceinline__
#define KVAUX 17
#define LAS __attribute__((address_space(3)))
#define GAS __attribute__((address_space(1)))

typedef __attribute__((ext_vector_type(8))) __bf16 bf16x8;
typedef __attribute__((ext_vector_type(2))) __bf16 bf16x2;
typedef __attribute__((ext_vector_type(2))) float f32x2;
typedef __attribute__((ext_vector_type(4))) float f32x4;
typedef __attribute__((ext_vector_type(4))) unsigned u32x4;
typedef __attribute__((ext_vector_type(2))) unsigned u32x2;
typedef __attribute__((ext_vector_type(4))) short s16x4;
typedef unsigned long long u64;
typedef GAS u64 gu64;
typedef __amdgpu_buffer_rsrc_t rsrc_t;

namespace {
constexpr int XROW = 896;
constexpr int WROW = 2688;
constexpr int L_WO = 62592;
constexpr int LDS_BYTES = 163840;
constexpr unsigned WS_FLAG = 4096;
constexpr int F_KV = 0, F_PQ = 1024, F_OB = 2048, F_RED = 3072;
constexpr unsigned WS_PART = 1u << 20;
constexpr unsigned WS_KVF = 2u << 20;
constexpr unsigned WS_PQ = 4u << 20;
constexpr unsigned WS_OB = 6u << 20;
constexpr unsigned WS_BYTES = 8u << 20;
constexpr unsigned SPIN_CAP = 1u << 20;
}

DI unsigned pk2(float a, float b) { f32x2 p = {a, b}; return __builtin_bit_cast(unsigned, __builtin_convertvector(p, bf16x2)); }
DI float bfr(float a) { return (float)(__bf16)a; }
DI float lo16(unsigned u) { return __builtin_bit_cast(float, u << 16); }
DI float hi16(unsigned u) { return __builtin_bit_cast(float, u & 0xffff0000u); }
DI unsigned phi2(float a, float b) {
  unsigned u = pk2(a, b);
  const float t0 = lo16(u), t1 = hi16(u);
  const float e0 = __builtin_amdgcn_exp2f(t0 * 1.4426950408889634f), e1 = __builtin_amdgcn_exp2f(t1 * 1.4426950408889634f);
  u = pk2(t0 > 0.f ? t0 + 1.f : e0, t1 > 0.f ? t1 + 1.f : e1);
  return pk2(lo16(u) + 1e-6f, hi16(u) + 1e-6f);
}
DI unsigned rmul2(float a, float b, float s) { const unsigned u = pk2(a, b); return pk2(lo16(u) * s, hi16(u) * s); }
DI f32x4 mm(u32x4 a, u32x4 b, f32x4 c) {
  return __builtin_amdgcn_mfma_f32_16x16x32_bf16(__builtin_bit_cast(bf16x8, a), __builtin_bit_cast(bf16x8, b), c, 0, 0, 0);
}
DI u32x4 trf(const LAS char* p0, const LAS char* p1) {
  const u32x2 a = __builtin_bit_cast(u32x2, __builtin_amdgcn_ds_read_tr16_b64_v4i16((LAS s16x4*)p0));
  const u32x2 b = __builtin_bit_cast(u32x2, __builtin_amdgcn_ds_read_tr16_b64_v4i16((LAS s16x4*)p1));
  u32x4 r; r[0] = a[0]; r[1] = a[1]; r[2] = b[0]; r[3] = b[1]; return r;
}
DI void glds(unsigned m0v, unsigned voff, rsrc_t rs, unsigned soff) {
  asm volatile("s_mov_b32 m0, %0\n\ts_nop 0\n\tbuffer_load_dwordx4 %1, %2, %3 offen lds"
               :: "s"(m0v), "v"(voff), "s"(rs), "s"(soff) : "m0", "memory");
}
#define GLD16(dst, ptr, off) asm volatile("global_load_dwordx4 %0, %1, off offset:%2" : "=v"(dst) : "v"(ptr), "i"(off) : "memory")
#define VMWAIT(n) asm volatile("s_waitcnt vmcnt(%0)" :: "i"(n) : "memory")
#define LGWAIT() asm volatile("s_waitcnt lgkmcnt(0)" ::: "memory")
#define TIE(v) asm volatile("" : "+v"(v))
#define WSTAMP(k) do {} while (0)
#define STAMP(k) do {} while (0)
DI void bar() { asm volatile("" ::: "memory"); __builtin_amdgcn_s_barrier(); asm volatile("" ::: "memory"); }
DI void st_sc1(u32x4 v, rsrc_t rs, unsigned off) { __builtin_amdgcn_raw_buffer_store_b128(v, rs, off, 0, 16); }
DI u32x4 ld_sc1(rsrc_t rs, unsigned off) { return __builtin_amdgcn_raw_buffer_load_b128(rs, off, 0, 16); }
DI void set_flag(char* ws, int idx, u64 tag) {
  __hip_atomic_store((gu64*)(ws + WS_FLAG + idx * 64), tag, __ATOMIC_RELAXED, __HIP_MEMORY_SCOPE_AGENT);
}
DI void wait_flags(char* ws, int idx, u64 tag) {
  const int ii = idx < 0 ? 0 : idx;
  for (unsigned spins = 0; spins < SPIN_CAP; ++spins) {
    const u64 v = __hip_atomic_load((gu64*)(ws + WS_FLAG + ii * 64), __ATOMIC_RELAXED, __HIP_MEMORY_SCOPE_AGENT);
    if (__all(idx < 0 || v == tag)) break;
    __builtin_amdgcn_s_sleep(1);
  }
  __builtin_amdgcn_fence(__ATOMIC_ACQUIRE, "wavefront");
}

DI void reduce_job(char* ws, rsrc_t wsr, int job, int lane, u64 tag) {
  const int w = 0;
  const int h = job >> 1, u = job & 1;
  wait_flags(ws, lane < 32 ? F_KV + ((lane >> 1) * 8 + (h >> 1)) * 8 + (h & 1) * 4 + 2 * u + (lane & 1) : -1, tag);
  STAMP(17);
  u32x4 v[16][2];
#pragma unroll
  for (int c = 0; c < 16; ++c)
#pragma unroll
    for (int t = 0; t < 2; ++t) v[c][t] = ld_sc1(wsr, WS_PART + (unsigned)(((c * 16 + h) * 4 + u * 2 + t) * 1024 + lane * 16));
  f32x4 s0 = __builtin_bit_cast(f32x4, v[0][0]), s1 = __builtin_bit_cast(f32x4, v[0][1]);
#pragma unroll
  for (int c = 1; c < 16; ++c) { s0 += __builtin_bit_cast(f32x4, v[c][0]); s1 += __builtin_bit_cast(f32x4, v[c][1]); }
  u32x4 o; o[0] = pk2(s0[0], s0[1]); o[1] = pk2(s0[2], s0[3]); o[2] = pk2(s1[0], s1[1]); o[3] = pk2(s1[2], s1[3]);
  STAMP(18);
  st_sc1(o, wsr, WS_KVF + (unsigned)(job * 1024 + lane * 16));
}

constexpr int A_R = 114688;
constexpr int B_SLOT = 39936, B_OX = 21504, B_OH = 37888;
DI constexpr int a_qbase(int ks) { return A_R + (ks < 4 ? 32768 + ks * 3584 : (ks < 13 ? (ks - 4) * 3584 : 32768)); }

template <bool IS_A>
DI void loader(const char* x, const char* W, const char* wo, LAS char* lds, unsigned ldsb, int chunk, int hp, int cg, int w, int lane) {
  const int r0 = chunk * 128;
  if (w == 8) {
    const rsrc_t wr = __builtin_amdgcn_make_buffer_rsrc((void*)W, 0, 448 * WROW, 0x00020000);
    const int sl = lane / 7, sg = lane - sl * 7;
    const unsigned vo0 = (unsigned)((4 * (sl & 1) + (sl >> 1)) * WROW + sg * 16), vo1 = vo0 + 8 * WROW;
    const unsigned so = (unsigned)(hp * 112);
    auto piece = [&](unsigned dst, int part, int j) {
      if (lane < 56) glds(dst, (j & 1) ? vo1 : vo0, wr, so + part * 896 + (j >> 1) * 16 * WROW);
    };
    if constexpr (IS_A) {
      auto stage = [&](int st) {
#pragma unroll
        for (int j = 0; j < 8; ++j) {
          piece(ldsb + A_R + (st % 3) * 14336 + j * 896, 1, st * 8 + j);
          piece(ldsb + A_R + (st % 3) * 14336 + 7168 + j * 896, 2, st * 8 + j);
        }
      };
      stage(0); stage(1); stage(2);
#pragma unroll
      for (int st = 0; st < 7; ++st) {
        if (st == 0) VMWAIT(32); else if (st < 6) VMWAIT(16); else VMWAIT(0);
        bar();
        if (st >= 1 && st + 2 <= 6) stage(st + 2);
      }
#pragma unroll
      for (int j = 0; j < 16; ++j) piece(ldsb + a_qbase(j >> 2) + (j & 3) * 896, 0, j);
      bar(); bar();
      VMWAIT(0); bar();
#pragma unroll
      for (int j = 16; j < 52; ++j) piece(ldsb + a_qbase(j >> 2) + (j & 3) * 896, 0, j);
      VMWAIT(0); bar();
#pragma unroll
      for (int j = 52; j < 56; ++j) piece(ldsb + a_qbase(j >> 2) + (j & 3) * 896, 0, j);
      VMWAIT(0); bar();
      bar();
    } else {
      auto stage = [&](int st) {
#pragma unroll
        for (int j = 0; j < 8; ++j) {
          piece(ldsb + (st & 3) * B_SLOT + j * 896, 1, st * 8 + j);
          piece(ldsb + (st & 3) * B_SLOT + 7168 + j * 896, 2, st * 8 + j);
          piece(ldsb + (st & 3) * B_SLOT + 14336 + j * 896, 0, st * 8 + j);
        }
      };
      stage(0); stage(1);
#pragma unroll
      for (int st = 0; st < 7; ++st) {
        if (st < 6) VMWAIT(24); else VMWAIT(0);
        bar();
        if (st + 2 <= 6) stage(st + 2);
      }
      bar();
    }
  } else {
    const rsrc_t xr = __builtin_amdgcn_make_buffer_rsrc((void*)x, 0, 2048 * XROW, 0x00020000);
    const unsigned vo = (unsigned)((lane >> 3) * XROW + (((lane & 7) ^ (lane >> 3)) * 16));
    const int hbase = r0 - 16 < 0 ? 0 : r0 - 16;
    auto stage = [&](int st) {
      const unsigned dst = ldsb + (IS_A ? st * 16384 : (st & 3) * B_SLOT + B_OX);
#pragma unroll
      for (int j = 0; j < 16; ++j) glds(dst + j * 1024, vo, xr, (unsigned)((r0 + 8 * j) * XROW + st * 128));
      if constexpr (!IS_A) {
        glds(ldsb + (st & 3) * B_SLOT + B_OH, vo, xr, (unsigned)(hbase * XROW + st * 128));
        glds(ldsb + (st & 3) * B_SLOT + B_OH + 1024, vo, xr, (unsigned)((hbase + 8) * XROW + st * 128));
      }
    };
    constexpr int PER = IS_A ? 16 : 18;
    stage(0); stage(1); stage(2);
#pragma unroll
    for (int st = 0; st < 7; ++st) {
      if (st < 5) VMWAIT(2 * PER); else if (st == 5) VMWAIT(PER); else VMWAIT(0);
      bar();
      if (st + 3 <= 6) stage(st + 3);
    }
    bar();
    if constexpr (IS_A) { bar(); bar(); bar(); bar(); bar(); }
  }
  if constexpr (!IS_A) bar();
  bar();
  {
    const rsrc_t rs = __builtin_amdgcn_make_buffer_rsrc((void*)wo, 0, 448 * XROW, 0x00020000);
    const unsigned row = lane / 14, seg = lane - row * 14;
    const unsigned voff = row * XROW + seg * 16;
    if (lane < 56) {
#pragma unroll
      for (int j = 0; j < 56; ++j) {
        const unsigned n = 2 * j + (w & 1);
        glds(ldsb + L_WO + n * 896, voff, rs, n * 4 * XROW + cg * 224);
      }
      if (w == 8) { u32x4 z = {0, 0, 0, 0}; *(LAS u32x4*)(lds + L_WO + 448 * 224 + lane * 16) = z; }
    }
  }
  VMWAIT(0); LGWAIT(); bar();
  bar(); bar();
}

template <bool IS_A>
DI void phase1(char* ws, rsrc_t wsr, LAS char* lds, int chunk, int hp, int w, int lane, u64& tag, int job) {
  const int mg = w & 3, hh = w >> 2, g = lane >> 4, i = lane & 15;
  const int head = 2 * hp + hh;
  const int q = (lane >> 2) & 3, p = lane & 3;
  int toff[2][2];
#pragma unroll
  for (int t = 0; t < 2; ++t)
#pragma unroll
    for (int rd = 0; rd < 2; ++rd)
      toff[t][rd] = (16 * (g >> 1) + 2 * (4 * (g & 1) + q) + rd) * 112 + (28 * hh + 16 * t + 4 * p) * 2;
  int xoff[2];
#pragma unroll
  for (int kk = 0; kk < 2; ++kk) xoff[kk] = (32 * mg + i) * 128 + (((4 * kk + g) ^ (i & 7)) * 16);
  const f32x4 z4 = {0.f, 0.f, 0.f, 0.f};
#define WFR(base, t) trf(lds + (base) + toff[t][0], lds + (base) + toff[t][1])

  if constexpr (IS_A) {
#define KB(ks) (A_R + (((ks) >> 1) % 3) * 14336 + ((ks) & 1) * 3584)
#define XFR(ks, m) (*(const LAS u32x4*)(lds + ((ks) >> 1) * 16384 + xoff[(ks) & 1] + (m) * 2048))
    f32x4 ak[2][2] = {{z4, z4}, {z4, z4}}, av[2][2] = {{z4, z4}, {z4, z4}};
    u32x4 wk[2][2], wv[2][2], xf[2][2];
    STAMP(7);
    bar();
    STAMP(8);
#pragma unroll
    for (int t = 0; t < 2; ++t) { wk[0][t] = WFR(KB(0), t); wv[0][t] = WFR(KB(0) + 7168, t); xf[0][t] = XFR(0, t); }
#pragma unroll
    for (int ks = 0; ks < 14; ++ks) {
      if (ks + 1 < 14) {
        if (((ks + 1) & 1) == 0) { LGWAIT(); bar(); STAMP(8 + (ks + 1) / 2); }
#pragma unroll
        for (int t = 0; t < 2; ++t) {
          wk[(ks + 1) & 1][t] = WFR(KB(ks + 1), t); wv[(ks + 1) & 1][t] = WFR(KB(ks + 1) + 7168, t); xf[(ks + 1) & 1][t] = XFR(ks + 1, t);
        }
      }
#pragma unroll
      for (int t = 0; t < 2; ++t)
#pragma unroll
        for (int m = 0; m < 2; ++m) {
          ak[t][m] = mm(xf[ks & 1][m], wk[ks & 1][t], ak[t][m]);
          av[t][m] = mm(xf[ks & 1][m], wv[ks & 1][t], av[t][m]);
        }
    }
    STAMP(1); WSTAMP(4);
    u32x4 pkf[2], vf[2];
#pragma unroll
    for (int t = 0; t < 2; ++t) {
      const unsigned keep = ((t == 1) && (i >= 12)) ? 0u : ~0u;
      const unsigned one = ((t == 1) && (i == 12)) ? 0x3f803f80u : 0u;
#pragma unroll
      for (int m = 0; m < 2; ++m) {
        pkf[t][2 * m] = phi2(ak[t][m][0], ak[t][m][1]) & keep; pkf[t][2 * m + 1] = phi2(ak[t][m][2], ak[t][m][3]) & keep;
        vf[t][2 * m] = (pk2(av[t][m][0], av[t][m][1]) & keep) | one; vf[t][2 * m + 1] = (pk2(av[t][m][2], av[t][m][3]) & keep) | one;
      }
    }
    f32x4 kvp[2][2];
#pragma unroll
    for (int u = 0; u < 2; ++u)
#pragma unroll
      for (int t = 0; t < 2; ++t) kvp[u][t] = mm(pkf[t], vf[u], z4);
    bar();
#pragma unroll
    for (int u = 0; u < 2; ++u)
#pragma unroll
      for (int t = 0; t < 2; ++t) *(LAS f32x4*)(lds + A_R + (w * 4 + u * 2 + t) * 1024 + lane * 16) = kvp[u][t];
    LGWAIT(); bar();
    {
      f32x4 s = *(const LAS f32x4*)(lds + A_R + ((hh * 4 + 0) * 4 + mg) * 1024 + lane * 16);
#pragma unroll
      for (int j = 1; j < 4; ++j) s += *(const LAS f32x4*)(lds + A_R + ((hh * 4 + j) * 4 + mg) * 1024 + lane * 16);
      st_sc1(__builtin_bit_cast(u32x4, s), wsr, WS_PART + (unsigned)(((chunk * 16 + head) * 4 + mg) * 1024 + lane * 16));
    }
    LGWAIT(); bar();
    STAMP(2); WSTAMP(5);
    f32x4 aq[2][2] = {{z4, z4}, {z4, z4}};
    u32x4 wq[2][2];
#pragma unroll
    for (int t = 0; t < 2; ++t) { wq[0][t] = WFR(a_qbase(0), t); xf[0][t] = XFR(0, t); }
#pragma unroll
    for (int ks = 0; ks < 14; ++ks) {
      if (ks + 1 < 14) {
        if (ks + 1 == 4 || ks + 1 == 13) { LGWAIT(); bar(); }
        if (ks + 1 == 5) {
          VMWAIT(0); TIE(tag); tag += 1;
          if (lane == 0) set_flag(ws, F_KV + (chunk * 8 + hp) * 8 + w, tag);
          STAMP(16);
        }
#pragma unroll
        for (int t = 0; t < 2; ++t) { wq[(ks + 1) & 1][t] = WFR(a_qbase(ks + 1), t); xf[(ks + 1) & 1][t] = XFR(ks + 1, t); }
      }
#pragma unroll
      for (int t = 0; t < 2; ++t)
#pragma unroll
        for (int m = 0; m < 2; ++m) aq[t][m] = mm(wq[ks & 1][t], xf[ks & 1][m], aq[t][m]);
    }
    bar();
    if (job >= 0) reduce_job(ws, wsr, job, lane, tag);
#pragma unroll
    for (int m = 0; m < 2; ++m) {
      u32x4 o;
#pragma unroll
      for (int t = 0; t < 2; ++t) {
        const unsigned keep = ((t == 1) && (g == 3)) ? 0u : ~0u;
        o[2 * t] = phi2(aq[t][m][0], aq[t][m][1]) & keep; o[2 * t + 1] = phi2(aq[t][m][2], aq[t][m][3]) & keep;
      }
      st_sc1(o, wsr, WS_PQ + (unsigned)(((chunk * 8 + 2 * mg + m) * 16 + head) * 1024 + lane * 16));
    }
    VMWAIT(0);
    if (lane == 0) { set_flag(ws, F_PQ + (chunk * 8 + hp) * 8 + w, tag); if (job >= 0) set_flag(ws, F_RED + job, tag); }
    STAMP(3); WSTAMP(6);
    bar();
  } else {
#define SB(ks) ((((ks) >> 1) & 3) * B_SLOT)
#define XFB(ks, m) (*(const LAS u32x4*)(lds + SB(ks) + B_OX + xoff[(ks) & 1] + (m) * 2048))
    f32x4 aq[2][2] = {{z4, z4}, {z4, z4}}, ak[2][2] = {{z4, z4}, {z4, z4}}, av[2][2] = {{z4, z4}, {z4, z4}}, ah = z4;
    u32x4 wq[2][2], wk[2][2], wv[2][2], xf[2][2], hx[2], hw[2];
    const int hw0 = ((mg & 2) ? 7168 : 0) + ((mg & 1) ? toff[1][0] : toff[0][0]);
    const int hw1 = ((mg & 2) ? 7168 : 0) + ((mg & 1) ? toff[1][1] : toff[0][1]);
#define HWFR(ks) trf(lds + SB(ks) + ((ks) & 1) * 3584 + hw0, lds + SB(ks) + ((ks) & 1) * 3584 + hw1)
    int hoff[2];
#pragma unroll
    for (int kk = 0; kk < 2; ++kk) hoff[kk] = B_OH + i * 128 + (((4 * kk + g) ^ (i & 7)) * 16);
#define HFR(ks) (*(const LAS u32x4*)(lds + SB(ks) + hoff[(ks) & 1]))
    bar();
#pragma unroll
    for (int t = 0; t < 2; ++t) { wq[0][t] = WFR(SB(0) + 14336, t); wk[0][t] = WFR(SB(0), t); wv[0][t] = WFR(SB(0) + 7168, t); xf[0][t] = XFB(0, t); }
    hx[0] = HFR(0); hw[0] = HWFR(0);
#pragma unroll
    for (int ks = 0; ks < 14; ++ks) {
      if (ks + 1 < 14) {
        if (((ks + 1) & 1) == 0) { LGWAIT(); bar(); STAMP(8 + (ks + 1) / 2); }
        const int bb = SB(ks + 1) + ((ks + 1) & 1) * 3584;
#pragma unroll
        for (int t = 0; t < 2; ++t) {
          wq[(ks + 1) & 1][t] = WFR(bb + 14336, t); wk[(ks + 1) & 1][t] = WFR(bb, t); wv[(ks + 1) & 1][t] = WFR(bb + 7168, t);
          xf[(ks + 1) & 1][t] = XFB(ks + 1, t);
        }
        hx[(ks + 1) & 1] = HFR(ks + 1); hw[(ks + 1) & 1] = HWFR(ks + 1);
      }
#pragma unroll
      for (int t = 0; t < 2; ++t)
#pragma unroll
        for (int m = 0; m < 2; ++m) {
          aq[t][m] = mm(wq[ks & 1][t], xf[ks & 1][m], aq[t][m]);
          ak[t][m] = mm(wk[ks & 1][t], xf[ks & 1][m], ak[t][m]);
          av[t][m] = mm(xf[ks & 1][m], wv[ks & 1][t], av[t][m]);
        }
      ah = mm(hw[ks & 1], hx[ks & 1], ah);
    }
    STAMP(1); WSTAMP(4);
    u32x4 qf[2], kf[2];
    u32x2 vfr[2][2];
#pragma unroll
    for (int m = 0; m < 2; ++m) {
#pragma unroll
      for (int t = 0; t < 2; ++t) {
        const unsigned keep = ((t == 1) && (g == 3)) ? 0u : ~0u;
        qf[m][2 * t] = pk2(aq[t][m][0], aq[t][m][1]) & keep; qf[m][2 * t + 1] = pk2(aq[t][m][2], aq[t][m][3]) & keep;
        kf[m][2 * t] = pk2(ak[t][m][0], ak[t][m][1]) & keep; kf[m][2 * t + 1] = pk2(ak[t][m][2], ak[t][m][3]) & keep;
        vfr[t][m][0] = pk2(av[t][m][0], av[t][m][1]); vfr[t][m][1] = pk2(av[t][m][2], av[t][m][3]);
      }
    }
    bar();
    {
      if (mg < 3) {
        LAS char* e = lds + (hh * 4 + mg + 1) * 2048;
        *(LAS u32x4*)(e + lane * 16) = kf[1];
        *(LAS u32x2*)(e + 1024 + lane * 8) = vfr[0][1];
        *(LAS u32x2*)(e + 1536 + lane * 8) = vfr[1][1];
      }
      LAS char* e0 = lds + (hh * 4) * 2048;
      const unsigned keep = ((chunk == 0) || ((mg == 1) && (g == 3))) ? 0u : ~0u;
      u32x2 hv; hv[0] = pk2(ah[0], ah[1]) & keep; hv[1] = pk2(ah[2], ah[3]) & keep;
      if (mg & 2) {
        LAS unsigned short* vt = (LAS unsigned short*)(e0 + 1024 + (mg & 1) * 512) + ((i >> 2) * 16 + 4 * g) * 4 + (i & 3);
        vt[0] = (unsigned short)hv[0]; vt[4] = (unsigned short)(hv[0] >> 16); vt[8] = (unsigned short)hv[1]; vt[12] = (unsigned short)(hv[1] >> 16);
      } else *(LAS u32x2*)(e0 + lane * 16 + (mg & 1) * 8) = hv;
    }
    LGWAIT(); bar();
    STAMP(2); WSTAMP(5);
    u32x4 kprev; u32x2 vprev[2];
    {
      const LAS char* e = lds + (hh * 4 + mg) * 2048;
      kprev = *(const LAS u32x4*)(e + lane * 16);
      vprev[0] = *(const LAS u32x2*)(e + 1024 + lane * 8);
      vprev[1] = *(const LAS u32x2*)(e + 1536 + lane * 8);
    }
    const float scale = 0.18898223650461362f;
#pragma unroll
    for (int m = 0; m < 2; ++m) {
      const u32x4 kp = m ? kf[0] : kprev;
      const f32x4 sp = mm(kp, qf[m], z4), so = mm(kf[m], qf[m], z4);
      const bool first = (chunk == 0) && (mg == 0) && (m == 0);
      float l[8]; bool ok[8]; float mx = -3.0e38f;
#pragma unroll
      for (int r = 0; r < 4; ++r) {
        const int dp = i + 16 - (4 * g + r), dn = i - (4 * g + r);
        ok[r] = (dp <= 14) && !(dp & 1) && !first;
        ok[4 + r] = (dn >= 0) && (dn <= 14) && !(dn & 1);
        l[r] = bfr(bfr(sp[r]) * scale); l[4 + r] = bfr(bfr(so[r]) * scale);
        mx = fmaxf(mx, ok[r] ? l[r] : -3.0e38f); mx = fmaxf(mx, ok[4 + r] ? l[4 + r] : -3.0e38f);
      }
      mx = fmaxf(mx, __shfl_xor(mx, 16)); mx = fmaxf(mx, __shfl_xor(mx, 32));
      float sum = 0.f;
#pragma unroll
      for (int r = 0; r < 8; ++r) { l[r] = ok[r] ? __builtin_amdgcn_exp2f((l[r] - mx) * 1.4426950408889634f) : 0.f; sum += l[r]; }
      sum += __shfl_xor(sum, 16); sum += __shfl_xor(sum, 32);
      const float inv = 1.f / sum;
      u32x4 pf;
#pragma unroll
      for (int r = 0; r < 4; ++r) pf[r] = pk2(l[2 * r] * inv, l[2 * r + 1] * inv);
      u32x4 o;
#pragma unroll
      for (int u = 0; u < 2; ++u) {
        const u32x2 va = m ? vfr[u][0] : vprev[u], vb = vfr[u][m];
        u32x4 vv; vv[0] = va[0]; vv[1] = va[1]; vv[2] = vb[0]; vv[3] = vb[1];
        const f32x4 oo = mm(vv, pf, z4);
        const unsigned keep = ((u == 1) && (g == 3)) ? 0u : ~0u;
        o[2 * u] = pk2(oo[0], oo[1]) & keep; o[2 * u + 1] = pk2(oo[2], oo[3]) & keep;
      }
      st_sc1(o, wsr, WS_OB + (unsigned)(((chunk * 8 + 2 * mg + m) * 16 + head) * 1024 + lane * 16));
    }
    VMWAIT(0); TIE(tag); tag += 1;
    if (lane == 0) set_flag(ws, F_OB + (chunk * 8 + hp) * 8 + w, tag);
    STAMP(3); WSTAMP(6);
    bar();
  }
}

template <bool IS_A>
DI void phase2(char* out, char* ws, rsrc_t wsr, LAS char* lds, int tt, int cg, int w, int lane, u64 tag, bool updater) {
  const int g = lane >> 4, i = lane & 15, q = (lane >> 2) & 3, p = lane & 3;
  const int tile = tt * 4 + (w & 3), kh = w >> 2;
  const f32x4 z4 = {0.f, 0.f, 0.f, 0.f};
  {
    const int hd = 8 * kh + (lane & 7);
    wait_flags(ws, lane < 8 ? (IS_A ? F_PQ : F_OB) + ((tile >> 3) * 8 + (hd >> 1)) * 8 + (hd & 1) * 4 + ((tile & 7) >> 1) : -1, tag);
  }
  STAMP(20);
  u32x4 of[8];
#pragma unroll
  for (int h = 0; h < 8; ++h) of[h] = ld_sc1(wsr, (IS_A ? WS_PQ : WS_OB) + (unsigned)((tile * 16 + 8 * kh + h) * 1024 + lane * 16));
  if constexpr (IS_A) {
    wait_flags(ws, lane < 4 ? F_RED + 4 * w + lane : -1, tag);
    STAMP(21);
    u32x4 t4[4];
#pragma unroll
    for (int j = 0; j < 4; ++j) t4[j] = __builtin_amdgcn_raw_buffer_load_b128(wsr, WS_KVF + (unsigned)((4 * w + j) * 1024 + lane * 16), 0, KVAUX);
#pragma unroll
    for (int j = 0; j < 4; ++j) *(LAS u32x4*)(lds + (4 * w + j) * 1024 + lane * 16) = t4[j];
  }
  STAMP(5); WSTAMP(0);
  LGWAIT(); bar();
  STAMP(22); WSTAMP(1);
  const LAS char* b0 = lds + L_WO + (8 * kh * 28 + 4 * g + q) * 224 + 8 * p;
  u32x4 wf[2][7];
#pragma unroll
  for (int n = 0; n < 7; ++n) wf[0][n] = trf(b0 + n * 32, b0 + 16 * 224 + n * 32);
  if constexpr (IS_A) {
    f32x4 n0[8], n1[8];
    float den[8];
#pragma unroll
    for (int h = 0; h < 8; ++h) {
      const u32x4 k0 = *(const LAS u32x4*)(lds + ((8 * kh + h) * 2) * 1024 + lane * 16);
      const u32x4 k1 = *(const LAS u32x4*)(lds + ((8 * kh + h) * 2 + 1) * 1024 + lane * 16);
      n0[h] = mm(k0, of[h], z4); n1[h] = mm(k1, of[h], z4);
    }
#pragma unroll
    for (int h = 0; h < 8; ++h) den[h] = __shfl(n1[h][0], 48 + i);
    const unsigned keep = (g == 3) ? 0u : ~0u;
#pragma unroll
    for (int h = 0; h < 8; ++h) {
      const float inv = __builtin_amdgcn_rcpf(bfr(bfr(den[h]) + 1e-6f));
      u32x4 o;
      o[0] = rmul2(n0[h][0], n0[h][1], inv); o[1] = rmul2(n0[h][2], n0[h][3], inv);
      o[2] = rmul2(n1[h][0], n1[h][1], inv) & keep; o[3] = rmul2(n1[h][2], n1[h][3], inv) & keep;
      of[h] = o;
    }
  }
  f32x4 acc[7];
#pragma unroll
  for (int n = 0; n < 7; ++n) acc[n] = z4;
#pragma unroll
  for (int h = 0; h < 8; ++h) {
    if (h + 1 < 8) {
#pragma unroll
      for (int n = 0; n < 7; ++n) wf[(h + 1) & 1][n] = trf(b0 + (h + 1) * 28 * 224 + n * 32, b0 + ((h + 1) * 28 + 16) * 224 + n * 32);
    }
#pragma unroll
    for (int n = 0; n < 7; ++n) acc[n] = mm(wf[h & 1][n], of[h], acc[n]);
    __builtin_amdgcn_sched_barrier(0);
  }
  STAMP(23); WSTAMP(2);
  LAS char* ex = lds + 32768 + (w & 3) * 7168 + lane * 16;
  if (kh == 0) {
#pragma unroll
    for (int n = 4; n < 7; ++n) *(LAS f32x4*)(ex + n * 1024) = acc[n];
  } else {
#pragma unroll
    for (int n = 0; n < 4; ++n) *(LAS f32x4*)(ex + n * 1024) = acc[n];
  }
  LGWAIT(); bar();
  LAS char* stg = lds + ((w & 3) * 16 + i) * 240 + g * 8;
  if (kh == 0) {
#pragma unroll
    for (int n = 0; n < 4; ++n) {
      const f32x4 v = acc[n] + *(const LAS f32x4*)(ex + n * 1024);
      u32x2 o; o[0] = pk2(v[0], v[1]); o[1] = pk2(v[2], v[3]);
      *(LAS u32x2*)(stg + n * 32) = o;
    }
  } else {
#pragma unroll
    for (int n = 4; n < 7; ++n) {
      const f32x4 v = *(const LAS f32x4*)(ex + n * 1024) + acc[n];
      u32x2 o; o[0] = pk2(v[0], v[1]); o[1] = pk2(v[2], v[3]);
      *(LAS u32x2*)(stg + n * 32) = o;
    }
  }
  LGWAIT(); bar();
  if (lane < 56) {
    const int row = lane / 14, seg = lane - row * 14;
#pragma unroll
    for (int j = 0; j < 2; ++j) {
      const int r = (w * 2 + j) * 4 + row;
      const u32x4 v = *(const LAS u32x4*)(lds + r * 240 + seg * 16);
      *(u32x4*)(out + (size_t)(tt * 64 + r) * 1792 + (IS_A ? 0 : 896) + cg * 224 + seg * 16) = v;
    }
  }
  STAMP(6); WSTAMP(3);
  if (updater && w == 0) {
    wait_flags(ws, lane < 32 ? F_RED + lane : -1, tag);
    wait_flags(ws, F_OB + lane * 8, tag);
    wait_flags(ws, F_OB + (64 + lane) * 8, tag);
    if (lane == 0) __hip_atomic_store((gu64*)ws, tag, __ATOMIC_RELAXED, __HIP_MEMORY_SCOPE_AGENT);
  }
}

__global__ void __launch_bounds__(640, 3)
fused_kernel(const char* xA, const char* wA, const char* woA, const char* xB, const char* wB, const char* woB,
             char* out, char* ws) {
  __shared__ __attribute__((aligned(16))) char smem[LDS_BYTES];
  LAS char* lds = (LAS char*)smem;
  const unsigned ldsb = (unsigned)(uintptr_t)lds;
  const int lane = threadIdx.x & 63;
  const int w = __builtin_amdgcn_readfirstlane(threadIdx.x >> 6);
  const int b = blockIdx.x, xcd = b & 7, idx = b >> 3, xq = xcd >> 1;
  const bool isA = (xcd & 1) == 0;
  const int chunk = (xq & 1) * 8 + (idx & 7), hp = (xq >> 1) * 4 + (idx >> 3);
  const int tt = xq * 8 + (idx & 7), cg = idx >> 3;
  if (w >= 8) {
    if (isA) loader<true>(xA, wA, woA, lds, ldsb, chunk, hp, cg, w, lane);
    else     loader<false>(xB, wB, woB, lds, ldsb, chunk, hp, cg, w, lane);
    return;
  }
  const int ua = xq * 32 + idx;
  const int job = ((ua & 3) == 0 && w == 0) ? (ua >> 2) : -1;
  const rsrc_t wsr = __builtin_amdgcn_make_buffer_rsrc((void*)ws, 0, WS_BYTES, 0x00020000);
  u64 tag;
  asm volatile("global_load_dwordx2 %0, %1, off sc1" : "=v"(tag) : "v"(ws) : "memory");
  STAMP(0);
  if (isA) {
    phase1<true>(ws, wsr, lds, chunk, hp, w, lane, tag, job);
    phase2<true>(out, ws, wsr, lds, tt, cg, w, lane, tag, false);
  } else {
    phase1<false>(ws, wsr, lds, chunk, hp, w, lane, tag, -1);
    phase2<false>(out, ws, wsr, lds, tt, cg, w, lane, tag, b == 1);
  }
}

extern "C" void kernel_launch(void* const* d_in, const int* in_sizes, int n_in,
                              void* d_out, int out_size, void* d_ws, size_t ws_size,
                              hipStream_t stream) {
  fused_kernel<<<256, 640, 0, stream>>>((const char*)d_in[0], (const char*)d_in[1], (const char*)d_in[2],
                                        (const char*)d_in[3], (const char*)d_in[4], (const char*)d_in[5],
                                        (char*)d_out, (char*)d_ws);
}
```

```cpp
#include <hip/hip_runtime.h>
#include <hip/hip_bf16.h>
#include <stdint.h>

#define DI __device__ __forceinline__
#define KVAUX 17
#define LAS __attribute__((address_space(3)))
#define GAS __attribute__((address_space(1)))

typedef __attribute__((ext_vector_type(8))) __bf16 bf16x8;
typedef __attribute__((ext_vector_type(2))) __bf16 bf16x2;
typedef __attribute__((ext_vector_type(2))) float f32x2;
typedef __attribute__((ext_vector_type(4))) float f32x4;
typedef __attribute__((ext_vector_type(4))) unsigned u32x4;
typedef __attribute__((ext_vector_type(2))) unsigned u32x2;
typedef __attribute__((ext_vector_type(4))) short s16x4;
typedef unsigned long long u64;
typedef GAS u64 gu64;
typedef __amdgpu_buffer_rsrc_t rsrc_t;

namespace {
constexpr int XROW = 896;
constexpr int WROW = 2688;
constexpr int L_WO = 62592;
constexpr int LDS_BYTES = 163840;
constexpr unsigned WS_FLAG = 4096;
constexpr int F_KV = 0, F_PQ = 1024, F_OB = 2048, F_RED = 3072;
constexpr unsigned WS_PART = 1u << 20;
constexpr unsigned WS_KVF = 2u << 20;
constexpr unsigned WS_PQ = 4u << 20;
constexpr unsigned WS_OB = 6u << 20;
constexpr unsigned WS_BYTES = 8u << 20;
constexpr unsigned SPIN_CAP = 1u << 20;
}

DI unsigned pk2(float a, float b) { f32x2 p = {a, b}; return __builtin_bit_cast(unsigned, __builtin_convertvector(p, bf16x2)); }
DI float bfr(float a) { return (float)(__bf16)a; }
DI float lo16(unsigned u) { return __builtin_bit_cast(float, u << 16); }
DI float hi16(unsigned u) { return __builtin_bit_cast(float, u & 0xffff0000u); }
DI unsigned phi2(float a, float b) {
  unsigned u = pk2(a, b);
  const float t0 = lo16(u), t1 = hi16(u);
  const float e0 = __builtin_amdgcn_exp2f(t0 * 1.4426950408889634f), e1 = __builtin_amdgcn_exp2f(t1 * 1.4426950408889634f);
  u = pk2(t0 > 0.f ? t0 + 1.f : e0, t1 > 0.f ? t1 + 1.f : e1);
  return pk2(lo16(u) + 1e-6f, hi16(u) + 1e-6f);
}
DI unsigned rmul2(float a, float b, float s) { const unsigned u = pk2(a, b); return pk2(lo16(u) * s, hi16(u) * s); }
DI f32x4 mm(u32x4 a, u32x4 b, f32x4 c) {
  return __builtin_amdgcn_mfma_f32_16x16x32_bf16(__builtin_bit_cast(bf16x8, a), __builtin_bit_cast(bf16x8, b), c, 0, 0, 0);
}
DI u32x4 trf(const LAS char* p0, const LAS char* p1) {
  const u32x2 a = __builtin_bit_cast(u32x2, __builtin_amdgcn_ds_read_tr16_b64_v4i16((LAS s16x4*)p0));
  const u32x2 b = __builtin_bit_cast(u32x2, __builtin_amdgcn_ds_read_tr16_b64_v4i16((LAS s16x4*)p1));
  u32x4 r; r[0] = a[0]; r[1] = a[1]; r[2] = b[0]; r[3] = b[1]; return r;
}
DI void glds(unsigned m0v, unsigned voff, rsrc_t rs, unsigned soff) {
  asm volatile("s_mov_b32 m0, %0\n\ts_nop 0\n\tbuffer_load_dwordx4 %1, %2, %3 offen lds"
               :: "s"(m0v), "v"(voff), "s"(rs), "s"(soff) : "m0", "memory");
}
#define GLD16(dst, ptr, off) asm volatile("global_load_dwordx4 %0, %1, off offset:%2" : "=v"(dst) : "v"(ptr), "i"(off) : "memory")
#define VMWAIT(n) asm volatile("s_waitcnt vmcnt(%0)" :: "i"(n) : "memory")
#define LGWAIT() asm volatile("s_waitcnt lgkmcnt(0)" ::: "memory")
#define TIE(v) asm volatile("" : "+v"(v))
#define WSTAMP(k) do {} while (0)
#define STAMP(k) do {} while (0)
DI void bar() { asm volatile("" ::: "memory"); __builtin_amdgcn_s_barrier(); asm volatile("" ::: "memory"); }
DI void st_sc1(u32x4 v, rsrc_t rs, unsigned off) { __builtin_amdgcn_raw_buffer_store_b128(v, rs, off, 0, 16); }
DI u32x4 ld_sc1(rsrc_t rs, unsigned off) { return __builtin_amdgcn_raw_buffer_load_b128(rs, off, 0, 16); }
DI void set_flag(char* ws, int idx, u64 tag) {
  __hip_atomic_store((gu64*)(ws + WS_FLAG + idx * 64), tag, __ATOMIC_RELAXED, __HIP_MEMORY_SCOPE_AGENT);
}
DI void wait_flags(char* ws, int idx, u64 tag) {
  const int ii = idx < 0 ? 0 : idx;
  for (unsigned spins = 0; spins < SPIN_CAP; ++spins) {
    const u64 v = __hip_atomic_load((gu64*)(ws + WS_FLAG + ii * 64), __ATOMIC_RELAXED, __HIP_MEMORY_SCOPE_AGENT);
    if (__all(idx < 0 || v == tag)) break;
    __builtin_amdgcn_s_sleep(1);
  }
  __builtin_amdgcn_fence(__ATOMIC_ACQUIRE, "wavefront");
}

DI void reduce_job(char* ws, rsrc_t wsr, int job, int lane, u64 tag) {
  const int w = 0;
  const int h = job >> 1, u = job & 1;
  wait_flags(ws, lane < 32 ? F_KV + ((lane >> 1) * 8 + (h >> 1)) * 8 + (h & 1) * 4 + 2 * u + (lane & 1) : -1, tag);
  STAMP(17);
  u32x4 v[16][2];
#pragma unroll
  for (int c = 0; c < 16; ++c)
#pragma unroll
    for (int t = 0; t < 2; ++t) v[c][t] = ld_sc1(wsr, WS_PART + (unsigned)(((c * 16 + h) * 4 + u * 2 + t) * 1024 + lane * 16));
  f32x4 s0 = __builtin_bit_cast(f32x4, v[0][0]), s1 = __builtin_bit_cast(f32x4, v[0][1]);
#pragma unroll
  for (int c = 1; c < 16; ++c) { s0 += __builtin_bit_cast(f32x4, v[c][0]); s1 += __builtin_bit_cast(f32x4, v[c][1]); }
  u32x4 o; o[0] = pk2(s0[0], s0[1]); o[1] = pk2(s0[2], s0[3]); o[2] = pk2(s1[0], s1[1]); o[3] = pk2(s1[2], s1[3]);
  STAMP(18);
  st_sc1(o, wsr, WS_KVF + (unsigned)(job * 1024 + lane * 16));
}

constexpr int A_R = 114688;
constexpr int B_SLOT = 39936, B_OX = 21504, B_OH = 37888;
DI constexpr int a_qbase(int ks) { return A_R + (ks < 4 ? 32768 + ks * 3584 : (ks < 13 ? (ks - 4) * 3584 : 32768)); }

template <bool IS_A>
DI void loader(const char* x, const char* W, const char* wo, LAS char* lds, unsigned ldsb, int chunk, int hp, int cg, int w, int lane) {
  const int r0 = chunk * 128;
  const bool act = lane < 56;
  if (w == 8) {
    const rsrc_t wr = __builtin_amdgcn_make_buffer_rsrc((void*)W, 0, 448 * WROW, 0x00020000);
    const int sl = lane / 7, sg = lane - sl * 7;
    const unsigned vo0 = (unsigned)((4 * (sl & 1) + (sl >> 1)) * WROW + sg * 16), vo1 = vo0 + 8 * WROW;
    const unsigned so = (unsigned)(hp * 112);
    auto stage = [&](int st) {
      unsigned d = ldsb + (IS_A ? A_R + (st % 3) * 14336 : (st & 3) * B_SLOT), s2 = so + st * 64 * WROW;
#pragma unroll 1
      for (int jj = 0; jj < 4; ++jj) {
        if (act) {
          glds(d, vo0, wr, s2 + 896); glds(d + 7168, vo0, wr, s2 + 1792);
          if constexpr (!IS_A) glds(d + 14336, vo0, wr, s2);
          glds(d + 896, vo1, wr, s2 + 896); glds(d + 7168 + 896, vo1, wr, s2 + 1792);
          if constexpr (!IS_A) glds(d + 14336 + 896, vo1, wr, s2);
        }
        d += 1792; s2 += 16 * WROW;
      }
    };
    auto qgroups = [&](unsigned d, int g0, int n) {
      unsigned s2 = so + g0 * 16 * WROW;
#pragma unroll 1
      for (int jj = 0; jj < n; ++jj) { if (act) { glds(d, vo0, wr, s2); glds(d + 896, vo1, wr, s2); } d += 1792; s2 += 16 * WROW; }
    };
    if constexpr (IS_A) {
      stage(0); stage(1); stage(2);
      VMWAIT(32); bar();
#pragma unroll 1
      for (int st = 1; st < 5; ++st) { VMWAIT(16); bar(); stage(st + 2); }
      VMWAIT(16); bar();
      VMWAIT(0); bar();
      qgroups(ldsb + a_qbase(0), 0, 8);
      bar(); bar();
      VMWAIT(0); bar();
      qgroups(ldsb + a_qbase(4), 8, 18);
      VMWAIT(0); bar();
      qgroups(ldsb + a_qbase(13), 26, 2);
      VMWAIT(0); bar();
      bar();
    } else {
      stage(0); stage(1);
#pragma unroll 1
      for (int st = 0; st < 5; ++st) { VMWAIT(24); bar(); stage(st + 2); }
      VMWAIT(24); bar();
      VMWAIT(0); bar();
      bar();
    }
  } else {
    const rsrc_t xr = __builtin_amdgcn_make_buffer_rsrc((void*)x, 0, 2048 * XROW, 0x00020000);
    const unsigned vo = (unsigned)((lane >> 3) * XROW + (((lane & 7) ^ (lane >> 3)) * 16));
    const int hbase = r0 - 16 < 0 ? 0 : r0 - 16;
    auto stage = [&](int st) {
      unsigned d = ldsb + (IS_A ? st * 16384 : (st & 3) * B_SLOT + B_OX), s2 = (unsigned)(r0 * XROW + st * 128);
#pragma unroll 1
      for (int jj = 0; jj < 4; ++jj) {
        glds(d, vo, xr, s2); glds(d + 1024, vo, xr, s2 + 8 * XROW); glds(d + 2048, vo, xr, s2 + 16 * XROW); glds(d + 3072, vo, xr, s2 + 24 * XROW);
        d += 4096; s2 += 32 * XROW;
      }
      if constexpr (!IS_A) {
        glds(d, vo, xr, (unsigned)(hbase * XROW + st * 128));
        glds(d + 1024, vo, xr, (unsigned)((hbase + 8) * XROW + st * 128));
      }
    };
    constexpr int PER = IS_A ? 16 : 18;
    stage(0); stage(1); stage(2);
#pragma unroll 1
    for (int st = 0; st < 4; ++st) { VMWAIT(2 * PER); bar(); stage(st + 3); }
    VMWAIT(2 * PER); bar();
    VMWAIT(PER); bar();
    VMWAIT(0); bar();
    bar();
    if constexpr (IS_A) { bar(); bar(); bar(); bar(); bar(); }
  }
  if constexpr (!IS_A) bar();
  bar();
  {
    const rsrc_t rs = __builtin_amdgcn_make_buffer_rsrc((void*)wo, 0, 448 * XROW, 0x00020000);
    const unsigned row = lane / 14, seg = lane - row * 14;
    const unsigned voff = row * XROW + seg * 16;
    unsigned d = ldsb + L_WO + (w & 1) * 896, s2 = (w & 1) * 4 * XROW + cg * 224;
#pragma unroll 1
    for (int j = 0; j < 14; ++j) {
      if (act) { glds(d, voff, rs, s2); glds(d + 1792, voff, rs, s2 + 8 * XROW); glds(d + 3584, voff, rs, s2 + 16 * XROW); glds(d + 5376, voff, rs, s2 + 24 * XROW); }
      d += 7168; s2 += 32 * XROW;
    }
    if (act && w == 8) { u32x4 z = {0, 0, 0, 0}; *(LAS u32x4*)(lds + L_WO + 448 * 224 + lane * 16) = z; }
  }
  VMWAIT(0); LGWAIT(); bar();
  bar(); bar();
}

template <bool IS_A>
DI void phase1(char* ws, rsrc_t wsr, LAS char* lds, int chunk, int hp, int w, int lane, u64& tag, int job) {
  const int mg = w & 3, hh = w >> 2, g = lane >> 4, i = lane & 15;
  const int head = 2 * hp + hh;
  const int q = (lane >> 2) & 3, p = lane & 3;
  int toff[2][2];
#pragma unroll
  for (int t = 0; t < 2; ++t)
#pragma unroll
    for (int rd = 0; rd < 2; ++rd)
      toff[t][rd] = (16 * (g >> 1) + 2 * (4 * (g & 1) + q) + rd) * 112 + (28 * hh + 16 * t + 4 * p) * 2;
  int xoff[2];
#pragma unroll
  for (int kk = 0; kk < 2; ++kk) xoff[kk] = (32 * mg + i) * 128 + (((4 * kk + g) ^ (i & 7)) * 16);
  const f32x4 z4 = {0.f, 0.f, 0.f, 0.f};
#define WFR(base, t) trf(lds + (base) + toff[t][0], lds + (base) + toff[t][1])

  if constexpr (IS_A) {
#define KB(ks) (A_R + (((ks) >> 1) % 3) * 14336 + ((ks) & 1) * 3584)
#define XFR(ks, m) (*(const LAS u32x4*)(lds + ((ks) >> 1) * 16384 + xoff[(ks) & 1] + (m) * 2048))
    f32x4 ak[2][2] = {{z4, z4}, {z4, z4}}, av[2][2] = {{z4, z4}, {z4, z4}};
    u32x4 wk[2][2], wv[2][2], xf[2][2];
    STAMP(7);
    bar();
    STAMP(8);
#pragma unroll
    for (int t = 0; t < 2; ++t) { wk[0][t] = WFR(KB(0), t); wv[0][t] = WFR(KB(0) + 7168, t); xf[0][t] = XFR(0, t); }
#pragma unroll
    for (int ks = 0; ks < 14; ++ks) {
      if (ks + 1 < 14) {
        if (((ks + 1) & 1) == 0) { LGWAIT(); bar(); STAMP(8 + (ks + 1) / 2); }
#pragma unroll
        for (int t = 0; t < 2; ++t) {
          wk[(ks + 1) & 1][t] = WFR(KB(ks + 1), t); wv[(ks + 1) & 1][t] = WFR(KB(ks + 1) + 7168, t); xf[(ks + 1) & 1][t] = XFR(ks + 1, t);
        }
      }
#pragma unroll
      for (int t = 0; t < 2; ++t)
#pragma unroll
        for (int m = 0; m < 2; ++m) {
          ak[t][m] = mm(xf[ks & 1][m], wk[ks & 1][t], ak[t][m]);
          av[t][m] = mm(xf[ks & 1][m], wv[ks & 1][t], av[t][m]);
        }
    }
    STAMP(1); WSTAMP(4);
    u32x4 pkf[2], vf[2];
#pragma unroll
    for (int t = 0; t < 2; ++t) {
      const unsigned keep = ((t == 1) && (i >= 12)) ? 0u : ~0u;
      const unsigned one = ((t == 1) && (i == 12)) ? 0x3f803f80u : 0u;
#pragma unroll
      for (int m = 0; m < 2; ++m) {
        pkf[t][2 * m] = phi2(ak[t][m][0], ak[t][m][1]) & keep; pkf[t][2 * m + 1] = phi2(ak[t][m][2], ak[t][m][3]) & keep;
        vf[t][2 * m] = (pk2(av[t][m][0], av[t][m][1]) & keep) | one; vf[t][2 * m + 1] = (pk2(av[t][m][2], av[t][m][3]) & keep) | one;
      }
    }
    f32x4 kvp[2][2];
#pragma unroll
    for (int u = 0; u < 2; ++u)
#pragma unroll
      for (int t = 0; t < 2; ++t) kvp[u][t] = mm(pkf[t], vf[u], z4);
    bar();
#pragma unroll
    for (int u = 0; u < 2; ++u)
#pragma unroll
      for (int t = 0; t < 2; ++t) *(LAS f32x4*)(lds + A_R + (w * 4 + u * 2 + t) * 1024 + lane * 16) = kvp[u][t];
    LGWAIT(); bar();
    {
      f32x4 s = *(const LAS f32x4*)(lds + A_R + ((hh * 4 + 0) * 4 + mg) * 1024 + lane * 16);
#pragma unroll
      for (int j = 1; j < 4; ++j) s += *(const LAS f32x4*)(lds + A_R + ((hh * 4 + j) * 4 + mg) * 1024 + lane * 16);
      st_sc1(__builtin_bit_cast(u32x4, s), wsr, WS_PART + (unsigned)(((chunk * 16 + head) * 4 + mg) * 1024 + lane * 16));
    }
    LGWAIT(); bar();
    STAMP(2); WSTAMP(5);
    f32x4 aq[2][2] = {{z4, z4}, {z4, z4}};
    u32x4 wq[2][2];
#pragma unroll
    for (int t = 0; t < 2; ++t) { wq[0][t] = WFR(a_qbase(0), t); xf[0][t] = XFR(0, t); }
#pragma unroll
    for (int ks = 0; ks < 14; ++ks) {
      if (ks + 1 < 14) {
        if (ks + 1 == 4 || ks + 1 == 13) { LGWAIT(); bar(); }
        if (ks + 1 == 5) {
          VMWAIT(0); TIE(tag); tag += 1;
          if (lane == 0) set_flag(ws, F_KV + (chunk * 8 + hp) * 8 + w, tag);
          STAMP(16);
        }
#pragma unroll
        for (int t = 0; t < 2; ++t) { wq[(ks + 1) & 1][t] = WFR(a_qbase(ks + 1), t); xf[(ks + 1) & 1][t] = XFR(ks + 1, t); }
      }
#pragma unroll
      for (int t = 0; t < 2; ++t)
#pragma unroll
        for (int m = 0; m < 2; ++m) aq[t][m] = mm(wq[ks & 1][t], xf[ks & 1][m], aq[t][m]);
    }
    bar();
    if (job >= 0) reduce_job(ws, wsr, job, lane, tag);
#pragma unroll
    for (int m = 0; m < 2; ++m) {
      u32x4 o;
#pragma unroll
      for (int t = 0; t < 2; ++t) {
        const unsigned keep = ((t == 1) && (g == 3)) ? 0u : ~0u;
        o[2 * t] = phi2(aq[t][m][0], aq[t][m][1]) & keep; o[2 * t + 1] = phi2(aq[t][m][2], aq[t][m][3]) & keep;
      }
      st_sc1(o, wsr, WS_PQ + (unsigned)(((chunk * 8 + 2 * mg + m) * 16 + head) * 1024 + lane * 16));
    }
    VMWAIT(0);
    if (lane == 0) { set_flag(ws, F_PQ + (chunk * 8 + hp) * 8 + w, tag); if (job >= 0) set_flag(ws, F_RED + job, tag); }
    STAMP(3); WSTAMP(6);
    bar();
  } else {
#define SB(ks) ((((ks) >> 1) & 3) * B_SLOT)
#define XFB(ks, m) (*(const LAS u32x4*)(lds + SB(ks) + B_OX + xoff[(ks) & 1] + (m) * 2048))
    f32x4 aq[2][2] = {{z4, z4}, {z4, z4}}, ak[2][2] = {{z4, z4}, {z4, z4}}, av[2][2] = {{z4, z4}, {z4, z4}}, ah = z4;
    u32x4 wq[2][2], wk[2][2], wv[2][2], xf[2][2], hx[2], hw[2];
    const int hw0 = ((mg & 2) ? 7168 : 0) + ((mg & 1) ? toff[1][0] : toff[0][0]);
    const int hw1 = ((mg & 2) ? 7168 : 0) + ((mg & 1) ? toff[1][1] : toff[0][1]);
#define HWFR(ks) trf(lds + SB(ks) + ((ks) & 1) * 3584 + hw0, lds + SB(ks) + ((ks) & 1) * 3584 + hw1)
    int hoff[2];
#pragma unroll
    for (int kk = 0; kk < 2; ++kk) hoff[kk] = B_OH + i * 128 + (((4 * kk + g) ^ (i & 7)) * 16);
#define HFR(ks) (*(const LAS u32x4*)(lds + SB(ks) + hoff[(ks) & 1]))
    bar();
#pragma unroll
    for (int t = 0; t < 2; ++t) { wq[0][t] = WFR(SB(0) + 14336, t); wk[0][t] = WFR(SB(0), t); wv[0][t] = WFR(SB(0) + 7168, t); xf[0][t] = XFB(0, t); }
    hx[0] = HFR(0); hw[0] = HWFR(0);
#pragma unroll
    for (int ks = 0; ks < 14; ++ks) {
      if (ks + 1 < 14) {
        if (((ks + 1) & 1) == 0) { LGWAIT(); bar(); STAMP(8 + (ks + 1) / 2); }
        const int bb = SB(ks + 1) + ((ks + 1) & 1) * 3584;
#pragma unroll
        for (int t = 0; t < 2; ++t) {
          wq[(ks + 1) & 1][t] = WFR(bb + 14336, t); wk[(ks + 1) & 1][t] = WFR(bb, t); wv[(ks + 1) & 1][t] = WFR(bb + 7168, t);
          xf[(ks + 1) & 1][t] = XFB(ks + 1, t);
        }
        hx[(ks + 1) & 1] = HFR(ks + 1); hw[(ks + 1) & 1] = HWFR(ks + 1);
      }
#pragma unroll
      for (int t = 0; t < 2; ++t)
#pragma unroll
        for (int m = 0; m < 2; ++m) {
          aq[t][m] = mm(wq[ks & 1][t], xf[ks & 1][m], aq[t][m]);
          ak[t][m] = mm(wk[ks & 1][t], xf[ks & 1][m], ak[t][m]);
          av[t][m] = mm(xf[ks & 1][m], wv[ks & 1][t], av[t][m]);
        }
      ah = mm(hw[ks & 1], hx[ks & 1], ah);
    }
    STAMP(1); WSTAMP(4);
    u32x4 qf[2], kf[2];
    u32x2 vfr[2][2];
#pragma unroll
    for (int m = 0; m < 2; ++m) {
#pragma unroll
      for (int t = 0; t < 2; ++t) {
        const unsigned keep = ((t == 1) && (g == 3)) ? 0u : ~0u;
        qf[m][2 * t] = pk2(aq[t][m][0], aq[t][m][1]) & keep; qf[m][2 * t + 1] = pk2(aq[t][m][2], aq[t][m][3]) & keep;
        kf[m][2 * t] = pk2(ak[t][m][0], ak[t][m][1]) & keep; kf[m][2 * t + 1] = pk2(ak[t][m][2], ak[t][m][3]) & keep;
        vfr[t][m][0] = pk2(av[t][m][0], av[t][m][1]); vfr[t][m][1] = pk2(av[t][m][2], av[t][m][3]);
      }
    }
    bar();
    {
      if (mg < 3) {
        LAS char* e = lds + (hh * 4 + mg + 1) * 2048;
        *(LAS u32x4*)(e + lane * 16) = kf[1];
        *(LAS u32x2*)(e + 1024 + lane * 8) = vfr[0][1];
        *(LAS u32x2*)(e + 1536 + lane * 8) = vfr[1][1];
      }
      LAS char* e0 = lds + (hh * 4) * 2048;
      const unsigned keep = ((chunk == 0) || ((mg == 1) && (g == 3))) ? 0u : ~0u;
      u32x2 hv; hv[0] = pk2(ah[0], ah[1]) & keep; hv[1] = pk2(ah[2], ah[3]) & keep;
      if (mg & 2) {
        LAS unsigned short* vt = (LAS unsigned short*)(e0 + 1024 + (mg & 1) * 512) + ((i >> 2) * 16 + 4 * g) * 4 + (i & 3);
        vt[0] = (unsigned short)hv[0]; vt[4] = (unsigned short)(hv[0] >> 16); vt[8] = (unsigned short)hv[1]; vt[12] = (unsigned short)(hv[1] >> 16);
      } else *(LAS u32x2*)(e0 + lane * 16 + (mg & 1) * 8) = hv;
    }
    LGWAIT(); bar();
    STAMP(2); WSTAMP(5);
    u32x4 kprev; u32x2 vprev[2];
    {
      const LAS char* e = lds + (hh * 4 + mg) * 2048;
      kprev = *(const LAS u32x4*)(e + lane * 16);
      vprev[0] = *(const LAS u32x2*)(e + 1024 + lane * 8);
      vprev[1] = *(const LAS u32x2*)(e + 1536 + lane * 8);
    }
    const float scale = 0.18898223650461362f;
#pragma unroll
    for (int m = 0; m < 2; ++m) {
      const u32x4 kp = m ? kf[0] : kprev;
      const f32x4 sp = mm(kp, qf[m], z4), so = mm(kf[m], qf[m], z4);
      const bool first = (chunk == 0) && (mg == 0) && (m == 0);
      float l[8]; bool ok[8]; float mx = -3.0e38f;
#pragma unroll
      for (int r = 0; r < 4; ++r) {
        const int dp = i + 16 - (4 * g + r), dn = i - (4 * g + r);
        ok[r] = (dp <= 14) && !(dp & 1) && !first;
        ok[4 + r] = (dn >= 0) && (dn <= 14) && !(dn & 1);
        l[r] = bfr(bfr(sp[r]) * scale); l[4 + r] = bfr(bfr(so[r]) * scale);
        mx = fmaxf(mx, ok[r] ? l[r] : -3.0e38f); mx = fmaxf(mx, ok[4 + r] ? l[4 + r] : -3.0e38f);
      }
      mx = fmaxf(mx, __shfl_xor(mx, 16)); mx = fmaxf(mx, __shfl_xor(mx, 32));
      float sum = 0.f;
#pragma unroll
      for (int r = 0; r < 8; ++r) { l[r] = ok[r] ? __builtin_amdgcn_exp2f((l[r] - mx) * 1.4426950408889634f) : 0.f; sum += l[r]; }
      sum += __shfl_xor(sum, 16); sum += __shfl_xor(sum, 32);
      const float inv = 1.f / sum;
      u32x4 pf;
#pragma unroll
      for (int r = 0; r < 4; ++r) pf[r] = pk2(l[2 * r] * inv, l[2 * r + 1] * inv);
      u32x4 o;
#pragma unroll
      for (int u = 0; u < 2; ++u) {
        const u32x2 va = m ? vfr[u][0] : vprev[u], vb = vfr[u][m];
        u32x4 vv; vv[0] = va[0]; vv[1] = va[1]; vv[2] = vb[0]; vv[3] = vb[1];
        const f32x4 oo = mm(vv, pf, z4);
        const unsigned keep = ((u == 1) && (g == 3)) ? 0u : ~0u;
        o[2 * u] = pk2(oo[0], oo[1]) & keep; o[2 * u + 1] = pk2(oo[2], oo[3]) & keep;
      }
      st_sc1(o, wsr, WS_OB + (unsigned)(((chunk * 8 + 2 * mg + m) * 16 + head) * 1024 + lane * 16));
    }
    VMWAIT(0); TIE(tag); tag += 1;
    if (lane == 0) set_flag(ws, F_OB + (chunk * 8 + hp) * 8 + w, tag);
    STAMP(3); WSTAMP(6);
    bar();
  }
}

template <bool IS_A>
DI void phase2(char* out, char* ws, rsrc_t wsr, LAS char* lds, int tt, int cg, int w, int lane, u64 tag, bool updater) {
  const int g = lane >> 4, i = lane & 15, q = (lane >> 2) & 3, p = lane & 3;
  const int tile = tt * 4 + (w & 3), kh = w >> 2;
  const f32x4 z4 = {0.f, 0.f, 0.f, 0.f};
  {
    const int hd = 8 * kh + (lane & 7);
    wait_flags(ws, lane < 8 ? (IS_A ? F_PQ : F_OB) + ((tile >> 3) * 8 + (hd >> 1)) * 8 + (hd & 1) * 4 + ((tile & 7) >> 1) : -1, tag);
  }
  STAMP(20);
  u32x4 of[8];
#pragma unroll
  for (int h = 0; h < 8; ++h) of[h] = ld_sc1(wsr, (IS_A ? WS_PQ : WS_OB) + (unsigned)((tile * 16 + 8 * kh + h) * 1024 + lane * 16));
  if constexpr (IS_A) {
    wait_flags(ws, lane < 4 ? F_RED + 4 * w + lane : -1, tag);
    STAMP(21);
    u32x4 t4[4];
#pragma unroll
    for (int j = 0; j < 4; ++j) t4[j] = __builtin_amdgcn_raw_buffer_load_b128(wsr, WS_KVF + (unsigned)((4 * w + j) * 1024 + lane * 16), 0, KVAUX);
#pragma unroll
    for (int j = 0; j < 4; ++j) *(LAS u32x4*)(lds + (4 * w + j) * 1024 + lane * 16) = t4[j];
  }
  STAMP(5); WSTAMP(0);
  LGWAIT(); bar();
  STAMP(22); WSTAMP(1);
  const LAS char* b0 = lds + L_WO + (8 * kh * 28 + 4 * g + q) * 224 + 8 * p;
  u32x4 wf[2][7];
#pragma unroll
  for (int n = 0; n < 7; ++n) wf[0][n] = trf(b0 + n * 32, b0 + 16 * 224 + n * 32);
  if constexpr (IS_A) {
    f32x4 n0[8], n1[8];
    float den[8];
#pragma unroll
    for (int h = 0; h < 8; ++h) {
      const u32x4 k0 = *(const LAS u32x4*)(lds + ((8 * kh + h) * 2) * 1024 + lane * 16);
      const u32x4 k1 = *(const LAS u32x4*)(lds + ((8 * kh + h) * 2 + 1) * 1024 + lane * 16);
      n0[h] = mm(k0, of[h], z4); n1[h] = mm(k1, of[h], z4);
    }
#pragma unroll
    for (int h = 0; h < 8; ++h) den[h] = __shfl(n1[h][0], 48 + i);
    const unsigned keep = (g == 3) ? 0u : ~0u;
#pragma unroll
    for (int h = 0; h < 8; ++h) {
      const float inv = __builtin_amdgcn_rcpf(bfr(bfr(den[h]) + 1e-6f));
      u32x4 o;
      o[0] = rmul2(n0[h][0], n0[h][1], inv); o[1] = rmul2(n0[h][2], n0[h][3], inv);
      o[2] = rmul2(n1[h][0], n1[h][1], inv) & keep; o[3] = rmul2(n1[h][2], n1[h][3], inv) & keep;
      of[h] = o;
    }
  }
  f32x4 acc[7];
#pragma unroll
  for (int n = 0; n < 7; ++n) acc[n] = z4;
#pragma unroll
  for (int h = 0; h < 8; ++h) {
    if (h + 1 < 8) {
#pragma unroll
      for (int n = 0; n < 7; ++n) wf[(h + 1) & 1][n] = trf(b0 + (h + 1) * 28 * 224 + n * 32, b0 + ((h + 1) * 28 + 16) * 224 + n * 32);
    }
#pragma unroll
    for (int n = 0; n < 7; ++n) acc[n] = mm(wf[h & 1][n], of[h], acc[n]);
    __builtin_amdgcn_sched_barrier(0);
  }
  STAMP(23); WSTAMP(2);
  LAS char* ex = lds + 32768 + (w & 3) * 7168 + lane * 16;
  if (kh == 0) {
#pragma unroll
    for (int n = 4; n < 7; ++n) *(LAS f32x4*)(ex + n * 1024) = acc[n];
  } else {
#pragma unroll
    for (int n = 0; n < 4; ++n) *(LAS f32x4*)(ex + n * 1024) = acc[n];
  }
  LGWAIT(); bar();
  LAS char* stg = lds + ((w & 3) * 16 + i) * 240 + g * 8;
  if (kh == 0) {
#pragma unroll
    for (int n = 0; n < 4; ++n) {
      const f32x4 v = acc[n] + *(const LAS f32x4*)(ex + n * 1024);
      u32x2 o; o[0] = pk2(v[0], v[1]); o[1] = pk2(v[2], v[3]);
      *(LAS u32x2*)(stg + n * 32) = o;
    }
  } else {
#pragma unroll
    for (int n = 4; n < 7; ++n) {
      const f32x4 v = *(const LAS f32x4*)(ex + n * 1024) + acc[n];
      u32x2 o; o[0] = pk2(v[0], v[1]); o[1] = pk2(v[2], v[3]);
      *(LAS u32x2*)(stg + n * 32) = o;
    }
  }
  LGWAIT(); bar();
  if (lane < 56) {
    const int row = lane / 14, seg = lane - row * 14;
#pragma unroll
    for (int j = 0; j < 2; ++j) {
      const int r = (w * 2 + j) * 4 + row;
      const u32x4 v = *(const LAS u32x4*)(lds + r * 240 + seg * 16);
      *(u32x4*)(out + (size_t)(tt * 64 + r) * 1792 + (IS_A ? 0 : 896) + cg * 224 + seg * 16) = v;
    }
  }
  STAMP(6); WSTAMP(3);
  if (updater && w == 0) {
    wait_flags(ws, lane < 32 ? F_RED + lane : -1, tag);
    wait_flags(ws, F_OB + lane * 8, tag);
    wait_flags(ws, F_OB + (64 + lane) * 8, tag);
    if (lane == 0) __hip_atomic_store((gu64*)ws, tag, __ATOMIC_RELAXED, __HIP_MEMORY_SCOPE_AGENT);
  }
}

__global__ void __launch_bounds__(640, 3)
fused_kernel(const char* xA, const char* wA, const char* woA, const char* xB, const char* wB, const char* woB,
             char* out, char* ws) {
  __shared__ __attribute__((aligned(16))) char smem[LDS_BYTES];
  LAS char* lds = (LAS char*)smem;
  const unsigned ldsb = (unsigned)(uintptr_t)lds;
  const int lane = threadIdx.x & 63;
  const int w = __builtin_amdgcn_readfirstlane(threadIdx.x >> 6);
  const int b = blockIdx.x, xcd = b & 7, idx = b >> 3, xq = xcd >> 1;
  const bool isA = (xcd & 1) == 0;
  const int chunk = (xq & 1) * 8 + (idx & 7), hp = (xq >> 1) * 4 + (idx >> 3);
  const int tt = xq * 8 + (idx & 7), cg = idx >> 3;
  if (w >= 8) {
    if (isA) loader<true>(xA, wA, woA, lds, ldsb, chunk, hp, cg, w, lane);
    else     loader<false>(xB, wB, woB, lds, ldsb, chunk, hp, cg, w, lane);
    return;
  }
  const int ua = xq * 32 + idx;
  const int job = ((ua & 3) == 0 && w == 0) ? (ua >> 2) : -1;
  const rsrc_t wsr = __builtin_amdgcn_make_buffer_rsrc((void*)ws, 0, WS_BYTES, 0x00020000);
  u64 tag;
  asm volatile("global_load_dwordx2 %0, %1, off sc1" : "=v"(tag) : "v"(ws) : "memory");
  STAMP(0);
  if (isA) {
    phase1<true>(ws, wsr, lds, chunk, hp, w, lane, tag, job);
    phase2<true>(out, ws, wsr, lds, tt, cg, w, lane, tag, false);
  } else {
    phase1<false>(ws, wsr, lds, chunk, hp, w, lane, tag, -1);
    phase2<false>(out, ws, wsr, lds, tt, cg, w, lane, tag, b == 1);
  }
}

extern "C" void kernel_launch(void* const* d_in, const int* in_sizes, int n_in,
                              void* d_out, int out_size, void* d_ws, size_t ws_size,
                              hipStream_t stream) {
  fused_kernel<<<256, 640, 0, stream>>>((const char*)d_in[0], (const char*)d_in[1], (const char*)d_in[2],
                                        (const char*)d_in[3], (const char*)d_in[4], (const char*)d_in[5],
                                        (char*)d_out, (char*)d_ws);
}
```

```cpp
#include <hip/hip_runtime.h>
#include <hip/hip_bf16.h>
#include <stdint.h>

#define DI __device__ __forceinline__
#define KVAUX 17
#define LAS __attribute__((address_space(3)))
#define GAS __attribute__((address_space(1)))

typedef __attribute__((ext_vector_type(8))) __bf16 bf16x8;
typedef __attribute__((ext_vector_type(2))) __bf16 bf16x2;
typedef __attribute__((ext_vector_type(2))) float f32x2;
typedef __attribute__((ext_vector_type(4))) float f32x4;
typedef __attribute__((ext_vector_type(4))) unsigned u32x4;
typedef __attribute__((ext_vector_type(2))) unsigned u32x2;
typedef __attribute__((ext_vector_type(4))) short s16x4;
typedef unsigned long long u64;
typedef GAS u64 gu64;
typedef __amdgpu_buffer_rsrc_t rsrc_t;

namespace {
constexpr int XROW = 896;
constexpr int WROW = 2688;
constexpr int L_WO = 62592;
constexpr int LDS_BYTES = 163840;
constexpr unsigned WS_FLAG = 4096;
constexpr int F_KV = 0, F_PQ = 1024, F_OB = 2048, F_RED = 3072;
constexpr unsigned WS_PART = 1u << 20;
constexpr unsigned WS_KVF = 2u << 20;
constexpr unsigned WS_PQ = 4u << 20;
constexpr unsigned WS_OB = 6u << 20;
constexpr unsigned WS_BYTES = 8u << 20;
constexpr unsigned SPIN_CAP = 1u << 20;
}

DI unsigned pk2(float a, float b) { f32x2 p = {a, b}; return __builtin_bit_cast(unsigned, __builtin_convertvector(p, bf16x2)); }
DI float bfr(float a) { return (float)(__bf16)a; }
DI float lo16(unsigned u) { return __builtin_bit_cast(float, u << 16); }
DI float hi16(unsigned u) { return __builtin_bit_cast(float, u & 0xffff0000u); }
DI unsigned phi2(float a, float b) {
  unsigned u = pk2(a, b);
  const float t0 = lo16(u), t1 = hi16(u);
  const float e0 = __builtin_amdgcn_exp2f(t0 * 1.4426950408889634f), e1 = __builtin_amdgcn_exp2f(t1 * 1.4426950408889634f);
  u = pk2(t0 > 0.f ? t0 + 1.f : e0, t1 > 0.f ? t1 + 1.f : e1);
  return pk2(lo16(u) + 1e-6f, hi16(u) + 1e-6f);
}
DI unsigned rmul2(float a, float b, float s) { const unsigned u = pk2(a, b); return pk2(lo16(u) * s, hi16(u) * s); }
DI f32x4 mm(u32x4 a, u32x4 b, f32x4 c) {
  return __builtin_amdgcn_mfma_f32_16x16x32_bf16(__builtin_bit_cast(bf16x8, a), __builtin_bit_cast(bf16x8, b), c, 0, 0, 0);
}
DI u32x4 trf(const LAS char* p0, const LAS char* p1) {
  const u32x2 a = __builtin_bit_cast(u32x2, __builtin_amdgcn_ds_read_tr16_b64_v4i16((LAS s16x4*)p0));
  const u32x2 b = __builtin_bit_cast(u32x2, __builtin_amdgcn_ds_read_tr16_b64_v4i16((LAS s16x4*)p1));
  u32x4 r; r[0] = a[0]; r[1] = a[1]; r[2] = b[0]; r[3] = b[1]; return r;
}
DI void glds(unsigned m0v, unsigned voff, rsrc_t rs, unsigned soff) {
  asm volatile("s_mov_b32 m0, %0\n\ts_nop 0\n\tbuffer_load_dwordx4 %1, %2, %3 offen lds"
               :: "s"(m0v), "v"(voff), "s"(rs), "s"(soff) : "m0", "memory");
}
#define GLD16(dst, ptr, off) asm volatile("global_load_dwordx4 %0, %1, off offset:%2" : "=v"(dst) : "v"(ptr), "i"(off) : "memory")
#define VMWAIT(n) asm volatile("s_waitcnt vmcnt(%0)" :: "i"(n) : "memory")
#define LGWAIT() asm volatile("s_waitcnt lgkmcnt(0)" ::: "memory")
#define TIE(v) asm volatile("" : "+v"(v))
#define WSTAMP(k) do {} while (0)
#define STAMP(k) do {} while (0)
DI void bar() { asm volatile("" ::: "memory"); __builtin_amdgcn_s_barrier(); asm volatile("" ::: "memory"); }
DI void st_sc1(u32x4 v, rsrc_t rs, unsigned off) { __builtin_amdgcn_raw_buffer_store_b128(v, rs, off, 0, 16); }
DI u32x4 ld_sc1(rsrc_t rs, unsigned off) { return __builtin_amdgcn_raw_buffer_load_b128(rs, off, 0, 16); }
DI void set_flag(char* ws, int idx, u64 tag) {
  __hip_atomic_store((gu64*)(ws + WS_FLAG + idx * 64), tag, __ATOMIC_RELAXED, __HIP_MEMORY_SCOPE_AGENT);
}
DI void wait_flags(char* ws, int idx, u64 tag) {
  const int ii = idx < 0 ? 0 : idx;
  for (unsigned spins = 0; spins < SPIN_CAP; ++spins) {
    const u64 v = __hip_atomic_load((gu64*)(ws + WS_FLAG + ii * 64), __ATOMIC_RELAXED, __HIP_MEMORY_SCOPE_AGENT);
    if (__all(idx < 0 || v == tag)) break;
    __builtin_amdgcn_s_sleep(1);
  }
  __builtin_amdgcn_fence(__ATOMIC_ACQUIRE, "wavefront");
}

DI void reduce_job(char* ws, rsrc_t wsr, int job, int lane, u64 tag) {
  const int w = 0;
  const int h = job >> 1, u = job & 1;
  wait_flags(ws, lane < 16 ? F_KV + (lane * 8 + (h >> 1)) * 8 + (h & 1) * 4 : -1, tag);
  STAMP(17);
  u32x4 v[16][2];
#pragma unroll
  for (int c = 0; c < 16; ++c)
#pragma unroll
    for (int t = 0; t < 2; ++t) v[c][t] = ld_sc1(wsr, WS_PART + (unsigned)(((c * 16 + h) * 4 + u * 2 + t) * 1024 + lane * 16));
  f32x4 s0 = __builtin_bit_cast(f32x4, v[0][0]), s1 = __builtin_bit_cast(f32x4, v[0][1]);
#pragma unroll
  for (int c = 1; c < 16; ++c) { s0 += __builtin_bit_cast(f32x4, v[c][0]); s1 += __builtin_bit_cast(f32x4, v[c][1]); }
  u32x4 o; o[0] = pk2(s0[0], s0[1]); o[1] = pk2(s0[2], s0[3]); o[2] = pk2(s1[0], s1[1]); o[3] = pk2(s1[2], s1[3]);
  STAMP(18);
  st_sc1(o, wsr, WS_KVF + (unsigned)(job * 1024 + lane * 16));
}

constexpr int A_R = 114688;
constexpr int B_SLOT = 39936, B_OX = 21504, B_OH = 37888;
DI constexpr int a_qbase(int ks) { return A_R + (ks < 9 ? 16384 + ks * 3584 : (ks < 13 ? (ks - 9) * 3584 : 16384)); }

template <bool IS_A>
DI void loader(const char* x, const char* W, const char* wo, char* ws, LAS char* lds, unsigned ldsb, int chunk, int hp, int cg, int w, int lane, int job) {
  const int r0 = chunk * 128;
  u64 tag = 0;
  if (job >= 0) asm volatile("global_load_dwordx2 %0, %1, off sc1" : "=v"(tag) : "v"(ws) : "memory");
  const bool act = lane < 56;
  if (w == 8) {
    const rsrc_t wr = __builtin_amdgcn_make_buffer_rsrc((void*)W, 0, 448 * WROW, 0x00020000);
    const int sl = lane / 7, sg = lane - sl * 7;
    const unsigned vo0 = (unsigned)((4 * (sl & 1) + (sl >> 1)) * WROW + sg * 16), vo1 = vo0 + 8 * WROW;
    const unsigned so = (unsigned)(hp * 112);
    auto stage = [&](int st) {
      unsigned d = ldsb + (IS_A ? A_R + (st % 3) * 14336 : (st & 3) * B_SLOT), s2 = so + st * 64 * WROW;
#pragma unroll 1
      for (int jj = 0; jj < 4; ++jj) {
        if (act) {
          glds(d, vo0, wr, s2 + 896); glds(d + 7168, vo0, wr, s2 + 1792);
          if constexpr (!IS_A) glds(d + 14336, vo0, wr, s2);
          glds(d + 896, vo1, wr, s2 + 896); glds(d + 7168 + 896, vo1, wr, s2 + 1792);
          if constexpr (!IS_A) glds(d + 14336 + 896, vo1, wr, s2);
        }
        d += 1792; s2 += 16 * WROW;
      }
    };
    auto qgroups = [&](unsigned d, int g0, int n) {
      unsigned s2 = so + g0 * 16 * WROW;
#pragma unroll 1
      for (int jj = 0; jj < n; ++jj) { if (act) { glds(d, vo0, wr, s2); glds(d + 896, vo1, wr, s2); } d += 1792; s2 += 16 * WROW; }
    };
    if constexpr (IS_A) {
      stage(0); stage(1); stage(2);
      VMWAIT(32); bar();
#pragma unroll 1
      for (int st = 1; st < 5; ++st) { VMWAIT(16); bar(); stage(st + 2); }
      VMWAIT(16); bar();
      qgroups(ldsb + a_qbase(0), 0, 6);
      VMWAIT(12); bar();
      qgroups(ldsb + a_qbase(3), 6, 12);
      bar(); bar();
      VMWAIT(0); bar();
      bar();
      qgroups(ldsb + a_qbase(9), 18, 8);
      VMWAIT(0); bar();
      qgroups(ldsb + a_qbase(13), 26, 2);
      VMWAIT(0); bar();
      bar();
    } else {
      stage(0); stage(1);
#pragma unroll 1
      for (int st = 0; st < 5; ++st) { VMWAIT(24); bar(); stage(st + 2); }
      VMWAIT(24); bar();
      VMWAIT(0); bar();
      bar();
    }
  } else {
    const rsrc_t xr = __builtin_amdgcn_make_buffer_rsrc((void*)x, 0, 2048 * XROW, 0x00020000);
    const unsigned vo = (unsigned)((lane >> 3) * XROW + (((lane & 7) ^ (lane >> 3)) * 16));
    const int hbase = r0 - 16 < 0 ? 0 : r0 - 16;
    auto stage = [&](int st) {
      unsigned d = ldsb + (IS_A ? st * 16384 : (st & 3) * B_SLOT + B_OX), s2 = (unsigned)(r0 * XROW + st * 128);
#pragma unroll 1
      for (int jj = 0; jj < 4; ++jj) {
        glds(d, vo, xr, s2); glds(d + 1024, vo, xr, s2 + 8 * XROW); glds(d + 2048, vo, xr, s2 + 16 * XROW); glds(d + 3072, vo, xr, s2 + 24 * XROW);
        d += 4096; s2 += 32 * XROW;
      }
      if constexpr (!IS_A) {
        glds(d, vo, xr, (unsigned)(hbase * XROW + st * 128));
        glds(d + 1024, vo, xr, (unsigned)((hbase + 8) * XROW + st * 128));
      }
    };
    constexpr int PER = IS_A ? 16 : 18;
    stage(0); stage(1); stage(2);
#pragma unroll 1
    for (int st = 0; st < 4; ++st) { VMWAIT(2 * PER); bar(); stage(st + 3); }
    VMWAIT(2 * PER); bar();
    VMWAIT(PER); bar();
    VMWAIT(0); bar();
    bar();
    if constexpr (IS_A) { bar(); bar(); bar(); bar(); bar(); bar(); }
  }
  if constexpr (!IS_A) {
    bar();
    if (job >= 0) {
      TIE(tag); tag += 1;
      const rsrc_t wsr = __builtin_amdgcn_make_buffer_rsrc((void*)ws, 0, WS_BYTES, 0x00020000);
      reduce_job(ws, wsr, job, lane, tag);
      VMWAIT(0);
      if (lane == 0) set_flag(ws, F_RED + job, tag);
    }
  }
  bar();
  {
    const rsrc_t rs = __builtin_amdgcn_make_buffer_rsrc((void*)wo, 0, 448 * XROW, 0x00020000);
    const unsigned row = lane / 14, seg = lane - row * 14;
    const unsigned voff = row * XROW + seg * 16;
    unsigned d = ldsb + L_WO + (w & 1) * 896, s2 = (w & 1) * 4 * XROW + cg * 224;
#pragma unroll 1
    for (int j = 0; j < 14; ++j) {
      if (act) { glds(d, voff, rs, s2); glds(d + 1792, voff, rs, s2 + 8 * XROW); glds(d + 3584, voff, rs, s2 + 16 * XROW); glds(d + 5376, voff, rs, s2 + 24 * XROW); }
      d += 7168; s2 += 32 * XROW;
    }
    if (act && w == 8) { u32x4 z = {0, 0, 0, 0}; *(LAS u32x4*)(lds + L_WO + 448 * 224 + lane * 16) = z; }
  }
  VMWAIT(0); LGWAIT(); bar();
  bar(); bar();
}

template <bool IS_A>
DI void phase1(char* ws, rsrc_t wsr, LAS char* lds, int chunk, int hp, int w, int lane, u64& tag, int job) {
  const int mg = w & 3, hh = w >> 2, g = lane >> 4, i = lane & 15;
  const int head = 2 * hp + hh;
  const int q = (lane >> 2) & 3, p = lane & 3;
  int toff[2][2];
#pragma unroll
  for (int t = 0; t < 2; ++t)
#pragma unroll
    for (int rd = 0; rd < 2; ++rd)
      toff[t][rd] = (16 * (g >> 1) + 2 * (4 * (g & 1) + q) + rd) * 112 + (28 * hh + 16 * t + 4 * p) * 2;
  int xoff[2];
#pragma unroll
  for (int kk = 0; kk < 2; ++kk) xoff[kk] = (32 * mg + i) * 128 + (((4 * kk + g) ^ (i & 7)) * 16);
  const f32x4 z4 = {0.f, 0.f, 0.f, 0.f};
#define WFR(base, t) trf(lds + (base) + toff[t][0], lds + (base) + toff[t][1])

  if constexpr (IS_A) {
#define KB(ks) (A_R + (((ks) >> 1) % 3) * 14336 + ((ks) & 1) * 3584)
#define XFR(ks, m) (*(const LAS u32x4*)(lds + ((ks) >> 1) * 16384 + xoff[(ks) & 1] + (m) * 2048))
    f32x4 ak[2][2] = {{z4, z4}, {z4, z4}}, av[2][2] = {{z4, z4}, {z4, z4}};
    u32x4 wk[2][2], wv[2][2], xf[2][2];
    STAMP(7);
    bar();
    STAMP(8);
#pragma unroll
    for (int t = 0; t < 2; ++t) { wk[0][t] = WFR(KB(0), t); wv[0][t] = WFR(KB(0) + 7168, t); xf[0][t] = XFR(0, t); }
#pragma unroll
    for (int ks = 0; ks < 14; ++ks) {
      if (ks + 1 < 14) {
        if (((ks + 1) & 1) == 0) { LGWAIT(); bar(); STAMP(8 + (ks + 1) / 2); }
#pragma unroll
        for (int t = 0; t < 2; ++t) {
          wk[(ks + 1) & 1][t] = WFR(KB(ks + 1), t); wv[(ks + 1) & 1][t] = WFR(KB(ks + 1) + 7168, t); xf[(ks + 1) & 1][t] = XFR(ks + 1, t);
        }
      }
#pragma unroll
      for (int t = 0; t < 2; ++t)
#pragma unroll
        for (int m = 0; m < 2; ++m) {
          ak[t][m] = mm(xf[ks & 1][m], wk[ks & 1][t], ak[t][m]);
          av[t][m] = mm(xf[ks & 1][m], wv[ks & 1][t], av[t][m]);
        }
    }
    STAMP(1); WSTAMP(4);
    u32x4 pkf[2], vf[2];
#pragma unroll
    for (int t = 0; t < 2; ++t) {
      const unsigned keep = ((t == 1) && (i >= 12)) ? 0u : ~0u;
      const unsigned one = ((t == 1) && (i == 12)) ? 0x3f803f80u : 0u;
#pragma unroll
      for (int m = 0; m < 2; ++m) {
        pkf[t][2 * m] = phi2(ak[t][m][0], ak[t][m][1]) & keep; pkf[t][2 * m + 1] = phi2(ak[t][m][2], ak[t][m][3]) & keep;
        vf[t][2 * m] = (pk2(av[t][m][0], av[t][m][1]) & keep) | one; vf[t][2 * m + 1] = (pk2(av[t][m][2], av[t][m][3]) & keep) | one;
      }
    }
    f32x4 kvp[2][2];
#pragma unroll
    for (int u = 0; u < 2; ++u)
#pragma unroll
      for (int t = 0; t < 2; ++t) kvp[u][t] = mm(pkf[t], vf[u], z4);
    bar();
    LAS char* xs = lds + A_R + (hh * 2 + (mg >> 1)) * 4096 + lane * 16;
    if (mg & 1) {
#pragma unroll
      for (int j = 0; j < 4; ++j) *(LAS f32x4*)(xs + j * 1024) = kvp[j >> 1][j & 1];
    }
    LGWAIT(); bar();
    if (!(mg & 1)) {
#pragma unroll
      for (int j = 0; j < 4; ++j) kvp[j >> 1][j & 1] += *(const LAS f32x4*)(xs + j * 1024);
      if (mg == 2) {
#pragma unroll
        for (int j = 0; j < 4; ++j) *(LAS f32x4*)(xs + j * 1024) = kvp[j >> 1][j & 1];
      }
    }
    LGWAIT(); bar();
    if (mg == 0) {
#pragma unroll
      for (int j = 0; j < 4; ++j) {
        const f32x4 s = kvp[j >> 1][j & 1] + *(const LAS f32x4*)(xs + 4096 + j * 1024);
        st_sc1(__builtin_bit_cast(u32x4, s), wsr, WS_PART + (unsigned)(((chunk * 16 + head) * 4 + j) * 1024 + lane * 16));
      }
    }
    STAMP(2); WSTAMP(5);
    f32x4 aq[2][2] = {{z4, z4}, {z4, z4}};
    u32x4 wq[2][2];
#pragma unroll
    for (int t = 0; t < 2; ++t) { wq[0][t] = WFR(a_qbase(0), t); xf[0][t] = XFR(0, t); }
#pragma unroll
    for (int ks = 0; ks < 14; ++ks) {
      if (ks + 1 < 14) {
        if (ks + 1 == 3 || ks + 1 == 9 || ks + 1 == 13) { LGWAIT(); bar(); }
        if (ks + 1 == 7) {
          VMWAIT(0); TIE(tag); tag += 1;
          if (mg == 0 && lane == 0) set_flag(ws, F_KV + (chunk * 8 + hp) * 8 + w, tag);
          STAMP(16);
        }
#pragma unroll
        for (int t = 0; t < 2; ++t) { wq[(ks + 1) & 1][t] = WFR(a_qbase(ks + 1), t); xf[(ks + 1) & 1][t] = XFR(ks + 1, t); }
      }
#pragma unroll
      for (int t = 0; t < 2; ++t)
#pragma unroll
        for (int m = 0; m < 2; ++m) aq[t][m] = mm(wq[ks & 1][t], xf[ks & 1][m], aq[t][m]);
    }
    bar();
    if (job >= 0) reduce_job(ws, wsr, job, lane, tag);
#pragma unroll
    for (int m = 0; m < 2; ++m) {
      u32x4 o;
#pragma unroll
      for (int t = 0; t < 2; ++t) {
        const unsigned keep = ((t == 1) && (g == 3)) ? 0u : ~0u;
        o[2 * t] = phi2(aq[t][m][0], aq[t][m][1]) & keep; o[2 * t + 1] = phi2(aq[t][m][2], aq[t][m][3]) & keep;
      }
      st_sc1(o, wsr, WS_PQ + (unsigned)(((chunk * 8 + 2 * mg + m) * 16 + head) * 1024 + lane * 16));
    }
    VMWAIT(0);
    if (lane == 0) { set_flag(ws, F_PQ + (chunk * 8 + hp) * 8 + w, tag); if (job >= 0) set_flag(ws, F_RED + job, tag); }
    STAMP(3); WSTAMP(6);
    bar();
  } else {
#define SB(ks) ((((ks) >> 1) & 3) * B_SLOT)
#define XFB(ks, m) (*(const LAS u32x4*)(lds + SB(ks) + B_OX + xoff[(ks) & 1] + (m) * 2048))
    f32x4 aq[2][2] = {{z4, z4}, {z4, z4}}, ak[2][2] = {{z4, z4}, {z4, z4}}, av[2][2] = {{z4, z4}, {z4, z4}}, ah = z4;
    u32x4 wq[2][2], wk[2][2], wv[2][2], xf[2][2], hx[2], hw[2];
    const int hw0 = ((mg & 2) ? 7168 : 0) + ((mg & 1) ? toff[1][0] : toff[0][0]);
    const int hw1 = ((mg & 2) ? 7168 : 0) + ((mg & 1) ? toff[1][1] : toff[0][1]);
#define HWFR(ks) trf(lds + SB(ks) + ((ks) & 1) * 3584 + hw0, lds + SB(ks) + ((ks) & 1) * 3584 + hw1)
    int hoff[2];
#pragma unroll
    for (int kk = 0; kk < 2; ++kk) hoff[kk] = B_OH + i * 128 + (((4 * kk + g) ^ (i & 7)) * 16);
#define HFR(ks) (*(const LAS u32x4*)(lds + SB(ks) + hoff[(ks) & 1]))
    bar();
#pragma unroll
    for (int t = 0; t < 2; ++t) { wq[0][t] = WFR(SB(0) + 14336, t); wk[0][t] = WFR(SB(0), t); wv[0][t] = WFR(SB(0) + 7168, t); xf[0][t] = XFB(0, t); }
    hx[0] = HFR(0); hw[0] = HWFR(0);
#pragma unroll
    for (int ks = 0; ks < 14; ++ks) {
      if (ks + 1 < 14) {
        if (((ks + 1) & 1) == 0) { LGWAIT(); bar(); STAMP(8 + (ks + 1) / 2); }
        const int bb = SB(ks + 1) + ((ks + 1) & 1) * 3584;
#pragma unroll
        for (int t = 0; t < 2; ++t) {
          wq[(ks + 1) & 1][t] = WFR(bb + 14336, t); wk[(ks + 1) & 1][t] = WFR(bb, t); wv[(ks + 1) & 1][t] = WFR(bb + 7168, t);
          xf[(ks + 1) & 1][t] = XFB(ks + 1, t);
        }
        hx[(ks + 1) & 1] = HFR(ks + 1); hw[(ks + 1) & 1] = HWFR(ks + 1);
      }
#pragma unroll
      for (int t = 0; t < 2; ++t)
#pragma unroll
        for (int m = 0; m < 2; ++m) {
          aq[t][m] = mm(wq[ks & 1][t], xf[ks & 1][m], aq[t][m]);
          ak[t][m] = mm(wk[ks & 1][t], xf[ks & 1][m], ak[t][m]);
          av[t][m] = mm(xf[ks & 1][m], wv[ks & 1][t], av[t][m]);
        }
      ah = mm(hw[ks & 1], hx[ks & 1], ah);
    }
    STAMP(1); WSTAMP(4);
    u32x4 qf[2], kf[2];
    u32x2 vfr[2][2];
#pragma unroll
    for (int m = 0; m < 2; ++m) {
#pragma unroll
      for (int t = 0; t < 2; ++t) {
        const unsigned keep = ((t == 1) && (g == 3)) ? 0u : ~0u;
        qf[m][2 * t] = pk2(aq[t][m][0], aq[t][m][1]) & keep; qf[m][2 * t + 1] = pk2(aq[t][m][2], aq[t][m][3]) & keep;
        kf[m][2 * t] = pk2(ak[t][m][0], ak[t][m][1]) & keep; kf[m][2 * t + 1] = pk2(ak[t][m][2], ak[t][m][3]) & keep;
        vfr[t][m][0] = pk2(av[t][m][0], av[t][m][1]); vfr[t][m][1] = pk2(av[t][m][2], av[t][m][3]);
      }
    }
    bar();
    {
      if (mg < 3) {
        LAS char* e = lds + (hh * 4 + mg + 1) * 2048;
        *(LAS u32x4*)(e + lane * 16) = kf[1];
        *(LAS u32x2*)(e + 1024 + lane * 8) = vfr[0][1];
        *(LAS u32x2*)(e + 1536 + lane * 8) = vfr[1][1];
      }
      LAS char* e0 = lds + (hh * 4) * 2048;
      const unsigned keep = ((chunk == 0) || ((mg == 1) && (g == 3))) ? 0u : ~0u;
      u32x2 hv; hv[0] = pk2(ah[0], ah[1]) & keep; hv[1] = pk2(ah[2], ah[3]) & keep;
      if (mg & 2) {
        LAS unsigned short* vt = (LAS unsigned short*)(e0 + 1024 + (mg & 1) * 512) + ((i >> 2) * 16 + 4 * g) * 4 + (i & 3);
        vt[0] = (unsigned short)hv[0]; vt[4] = (unsigned short)(hv[0] >> 16); vt[8] = (unsigned short)hv[1]; vt[12] = (unsigned short)(hv[1] >> 16);
      } else *(LAS u32x2*)(e0 + lane * 16 + (mg & 1) * 8) = hv;
    }
    LGWAIT(); bar();
    STAMP(2); WSTAMP(5);
    u32x4 kprev; u32x2 vprev[2];
    {
      const LAS char* e = lds + (hh * 4 + mg) * 2048;
      kprev = *(const LAS u32x4*)(e + lane * 16);
      vprev[0] = *(const LAS u32x2*)(e + 1024 + lane * 8);
      vprev[1] = *(const LAS u32x2*)(e + 1536 + lane * 8);
    }
    const float scale = 0.18898223650461362f;
#pragma unroll
    for (int m = 0; m < 2; ++m) {
      const u32x4 kp = m ? kf[0] : kprev;
      const f32x4 sp = mm(kp, qf[m], z4), so = mm(kf[m], qf[m], z4);
      const bool first = (chunk == 0) && (mg == 0) && (m == 0);
      float l[8]; bool ok[8]; float mx = -3.0e38f;
#pragma unroll
      for (int r = 0; r < 4; ++r) {
        const int dp = i + 16 - (4 * g + r), dn = i - (4 * g + r);
        ok[r] = (dp <= 14) && !(dp & 1) && !first;
        ok[4 + r] = (dn >= 0) && (dn <= 14) && !(dn & 1);
        l[r] = bfr(bfr(sp[r]) * scale); l[4 + r] = bfr(bfr(so[r]) * scale);
        mx = fmaxf(mx, ok[r] ? l[r] : -3.0e38f); mx = fmaxf(mx, ok[4 + r] ? l[4 + r] : -3.0e38f);
      }
      mx = fmaxf(mx, __shfl_xor(mx, 16)); mx = fmaxf(mx, __shfl_xor(mx, 32));
      float sum = 0.f;
#pragma unroll
      for (int r = 0; r < 8; ++r) { l[r] = ok[r] ? __builtin_amdgcn_exp2f((l[r] - mx) * 1.4426950408889634f) : 0.f; sum += l[r]; }
      sum += __shfl_xor(sum, 16); sum += __shfl_xor(sum, 32);
      const float inv = 1.f / sum;
      u32x4 pf;
#pragma unroll
      for (int r = 0; r < 4; ++r) pf[r] = pk2(l[2 * r] * inv, l[2 * r + 1] * inv);
      u32x4 o;
#pragma unroll
      for (int u = 0; u < 2; ++u) {
        const u32x2 va = m ? vfr[u][0] : vprev[u], vb = vfr[u][m];
        u32x4 vv; vv[0] = va[0]; vv[1] = va[1]; vv[2] = vb[0]; vv[3] = vb[1];
        const f32x4 oo = mm(vv, pf, z4);
        const unsigned keep = ((u == 1) && (g == 3)) ? 0u : ~0u;
        o[2 * u] = pk2(oo[0], oo[1]) & keep; o[2 * u + 1] = pk2(oo[2], oo[3]) & keep;
      }
      st_sc1(o, wsr, WS_OB + (unsigned)(((chunk * 8 + 2 * mg + m) * 16 + head) * 1024 + lane * 16));
    }
    VMWAIT(0); TIE(tag); tag += 1;
    if (lane == 0) set_flag(ws, F_OB + (chunk * 8 + hp) * 8 + w, tag);
    STAMP(3); WSTAMP(6);
    bar();
  }
}

template <bool IS_A>
DI void phase2(char* out, char* ws, rsrc_t wsr, LAS char* lds, int tt, int cg, int w, int lane, u64 tag, bool updater) {
  const int g = lane >> 4, i = lane & 15, q = (lane >> 2) & 3, p = lane & 3;
  const int tile = tt * 4 + (w & 3), kh = w >> 2;
  const f32x4 z4 = {0.f, 0.f, 0.f, 0.f};
  {
    const int hd = 8 * kh + (lane & 7);
    wait_flags(ws, lane < 8 ? (IS_A ? F_PQ : F_OB) + ((tile >> 3) * 8 + (hd >> 1)) * 8 + (hd & 1) * 4 + ((tile & 7) >> 1) : -1, tag);
  }
  STAMP(20);
  u32x4 of[8];
#pragma unroll
  for (int h = 0; h < 8; ++h) of[h] = ld_sc1(wsr, (IS_A ? WS_PQ : WS_OB) + (unsigned)((tile * 16 + 8 * kh + h) * 1024 + lane * 16));
  if constexpr (IS_A) {
    wait_flags(ws, lane < 4 ? F_RED + 4 * w + lane : -1, tag);
    STAMP(21);
    u32x4 t4[4];
#pragma unroll
    for (int j = 0; j < 4; ++j) t4[j] = __builtin_amdgcn_raw_buffer_load_b128(wsr, WS_KVF + (unsigned)((4 * w + j) * 1024 + lane * 16), 0, KVAUX);
#pragma unroll
    for (int j = 0; j < 4; ++j) *(LAS u32x4*)(lds + (4 * w + j) * 1024 + lane * 16) = t4[j];
  }
  STAMP(5); WSTAMP(0);
  LGWAIT(); bar();
  STAMP(22); WSTAMP(1);
  const LAS char* b0 = lds + L_WO + (8 * kh * 28 + 4 * g + q) * 224 + 8 * p;
  u32x4 wf[2][7];
#pragma unroll
  for (int n = 0; n < 7; ++n) wf[0][n] = trf(b0 + n * 32, b0 + 16 * 224 + n * 32);
  if constexpr (IS_A) {
    f32x4 n0[8], n1[8];
    float den[8];
#pragma unroll
    for (int h = 0; h < 8; ++h) {
      const u32x4 k0 = *(const LAS u32x4*)(lds + ((8 * kh + h) * 2) * 1024 + lane * 16);
      const u32x4 k1 = *(const LAS u32x4*)(lds + ((8 * kh + h) * 2 + 1) * 1024 + lane * 16);
      n0[h] = mm(k0, of[h], z4); n1[h] = mm(k1, of[h], z4);
    }
#pragma unroll
    for (int h = 0; h < 8; ++h) den[h] = __shfl(n1[h][0], 48 + i);
    const unsigned keep = (g == 3) ? 0u : ~0u;
#pragma unroll
    for (int h = 0; h < 8; ++h) {
      const float inv = __builtin_amdgcn_rcpf(bfr(bfr(den[h]) + 1e-6f));
      u32x4 o;
      o[0] = rmul2(n0[h][0], n0[h][1], inv); o[1] = rmul2(n0[h][2], n0[h][3], inv);
      o[2] = rmul2(n1[h][0], n1[h][1], inv) & keep; o[3] = rmul2(n1[h][2], n1[h][3], inv) & keep;
      of[h] = o;
    }
  }
  f32x4 acc[7];
#pragma unroll
  for (int n = 0; n < 7; ++n) acc[n] = z4;
#pragma unroll
  for (int h = 0; h < 8; ++h) {
    if (h + 1 < 8) {
#pragma unroll
      for (int n = 0; n < 7; ++n) wf[(h + 1) & 1][n] = trf(b0 + (h + 1) * 28 * 224 + n * 32, b0 + ((h + 1) * 28 + 16) * 224 + n * 32);
    }
#pragma unroll
    for (int n = 0; n < 7; ++n) acc[n] = mm(wf[h & 1][n], of[h], acc[n]);
    __builtin_amdgcn_sched_barrier(0);
  }
  STAMP(23); WSTAMP(2);
  LAS char* ex = lds + 32768 + (w & 3) * 7168 + lane * 16;
  if (kh == 0) {
#pragma unroll
    for (int n = 4; n < 7; ++n) *(LAS f32x4*)(ex + n * 1024) = acc[n];
  } else {
#pragma unroll
    for (int n = 0; n < 4; ++n) *(LAS f32x4*)(ex + n * 1024) = acc[n];
  }
  LGWAIT(); bar();
  LAS char* stg = lds + ((w & 3) * 16 + i) * 240 + g * 8;
  if (kh == 0) {
#pragma unroll
    for (int n = 0; n < 4; ++n) {
      const f32x4 v = acc[n] + *(const LAS f32x4*)(ex + n * 1024);
      u32x2 o; o[0] = pk2(v[0], v[1]); o[1] = pk2(v[2], v[3]);
      *(LAS u32x2*)(stg + n * 32) = o;
    }
  } else {
#pragma unroll
    for (int n = 4; n < 7; ++n) {
      const f32x4 v = *(const LAS f32x4*)(ex + n * 1024) + acc[n];
      u32x2 o; o[0] = pk2(v[0], v[1]); o[1] = pk2(v[2], v[3]);
      *(LAS u32x2*)(stg + n * 32) = o;
    }
  }
  LGWAIT(); bar();
  if (lane < 56) {
    const int row = lane / 14, seg = lane - row * 14;
#pragma unroll
    for (int j = 0; j < 2; ++j) {
      const int r = (w * 2 + j) * 4 + row;
      const u32x4 v = *(const LAS u32x4*)(lds + r * 240 + seg * 16);
      *(u32x4*)(out + (size_t)(tt * 64 + r) * 1792 + (IS_A ? 0 : 896) + cg * 224 + seg * 16) = v;
    }
  }
  STAMP(6); WSTAMP(3);
  if (updater && w == 0) {
    wait_flags(ws, lane < 32 ? F_RED + lane : -1, tag);
    wait_flags(ws, F_OB + lane * 8, tag);
    wait_flags(ws, F_OB + (64 + lane) * 8, tag);
    if (lane == 0) __hip_atomic_store((gu64*)ws, tag, __ATOMIC_RELAXED, __HIP_MEMORY_SCOPE_AGENT);
  }
}

__global__ void __launch_bounds__(640, 3)
fused_kernel(const char* xA, const char* wA, const char* woA, const char* xB, const char* wB, const char* woB,
             char* out, char* ws) {
  __shared__ __attribute__((aligned(16))) char smem[LDS_BYTES];
  LAS char* lds = (LAS char*)smem;
  const unsigned ldsb = (unsigned)(uintptr_t)lds;
  const int lane = threadIdx.x & 63;
  const int w = __builtin_amdgcn_readfirstlane(threadIdx.x >> 6);
  const int b = blockIdx.x, xcd = b & 7, idx = b >> 3, xq = xcd >> 1;
  const bool isA = (xcd & 1) == 0;
  const int chunk = (xq & 1) * 8 + (idx & 7), hp = (xq >> 1) * 4 + (idx >> 3);
  const int tt = xq * 8 + (idx & 7), cg = idx >> 3;
  if (w >= 8) {
    const int ub = xq * 32 + idx;
    if (isA) loader<true>(xA, wA, woA, ws, lds, ldsb, chunk, hp, cg, w, lane, -1);
    else     loader<false>(xB, wB, woB, ws, lds, ldsb, chunk, hp, cg, w, lane, ((ub & 3) == 2 && w == 9) ? (ub >> 2) : -1);
    return;
  }
  const int job = -1;
  const rsrc_t wsr = __builtin_amdgcn_make_buffer_rsrc((void*)ws, 0, WS_BYTES, 0x00020000);
  u64 tag;
  asm volatile("global_load_dwordx2 %0, %1, off sc1" : "=v"(tag) : "v"(ws) : "memory");
  STAMP(0);
  if (isA) {
    phase1<true>(ws, wsr, lds, chunk, hp, w, lane, tag, job);
    phase2<true>(out, ws, wsr, lds, tt, cg, w, lane, tag, false);
  } else {
    phase1<false>(ws, wsr, lds, chunk, hp, w, lane, tag, -1);
    phase2<false>(out, ws, wsr, lds, tt, cg, w, lane, tag, b == 1);
  }
}

extern "C" void kernel_launch(void* const* d_in, const int* in_sizes, int n_in,
                              void* d_out, int out_size, void* d_ws, size_t ws_size,
                              hipStream_t stream) {
  fused_kernel<<<256, 640, 0, stream>>>((const char*)d_in[0], (const char*)d_in[1], (const char*)d_in[2],
                                        (const char*)d_in[3], (const char*)d_in[4], (const char*)d_in[5],
                                        (char*)d_out, (char*)d_ws);
}
```

```cpp
#include <hip/hip_runtime.h>
#include <hip/hip_bf16.h>
#include <stdint.h>

#define DI __device__ __forceinline__
#define KVAUX 17
#define LAS __attribute__((address_space(3)))
#define GAS __attribute__((address_space(1)))

typedef __attribute__((ext_vector_type(8))) __bf16 bf16x8;
typedef __attribute__((ext_vector_type(2))) __bf16 bf16x2;
typedef __attribute__((ext_vector_type(2))) float f32x2;
typedef __attribute__((ext_vector_type(4))) float f32x4;
typedef __attribute__((ext_vector_type(4))) unsigned u32x4;
typedef __attribute__((ext_vector_type(2))) unsigned u32x2;
typedef __attribute__((ext_vector_type(4))) short s16x4;
typedef unsigned long long u64;
typedef GAS u64 gu64;
typedef __amdgpu_buffer_rsrc_t rsrc_t;

namespace {
constexpr int XROW = 896;
constexpr int WROW = 2688;
constexpr int L_WO = 62592;
constexpr int LDS_BYTES = 163840;
constexpr unsigned WS_FLAG = 4096;
constexpr int F_KV = 0, F_PQ = 1024, F_OB = 2048, F_RED = 3072;
constexpr unsigned WS_PART = 1u << 20;
constexpr unsigned WS_KVF = 2u << 20;
constexpr unsigned WS_PQ = 4u << 20;
constexpr unsigned WS_OB = 6u << 20;
constexpr unsigned WS_BYTES = 8u << 20;
constexpr unsigned SPIN_CAP = 1u << 20;
}

DI unsigned pk2(float a, float b) { f32x2 p = {a, b}; return __builtin_bit_cast(unsigned, __builtin_convertvector(p, bf16x2)); }
DI float bfr(float a) { return (float)(__bf16)a; }
DI float lo16(unsigned u) { return __builtin_bit_cast(float, u << 16); }
DI float hi16(unsigned u) { return __builtin_bit_cast(float, u & 0xffff0000u); }
DI unsigned phi2(float a, float b) {
  unsigned u = pk2(a, b);
  const float t0 = lo16(u), t1 = hi16(u);
  const float e0 = __builtin_amdgcn_exp2f(t0 * 1.4426950408889634f), e1 = __builtin_amdgcn_exp2f(t1 * 1.4426950408889634f);
  u = pk2(t0 > 0.f ? t0 + 1.f : e0, t1 > 0.f ? t1 + 1.f : e1);
  return pk2(lo16(u) + 1e-6f, hi16(u) + 1e-6f);
}
DI unsigned rmul2(float a, float b, float s) { const unsigned u = pk2(a, b); return pk2(lo16(u) * s, hi16(u) * s); }
DI f32x4 mm(u32x4 a, u32x4 b, f32x4 c) {
  return __builtin_amdgcn_mfma_f32_16x16x32_bf16(__builtin_bit_cast(bf16x8, a), __builtin_bit_cast(bf16x8, b), c, 0, 0, 0);
}
DI u32x4 trf(const LAS char* p0, const LAS char* p1) {
  const u32x2 a = __builtin_bit_cast(u32x2, __builtin_amdgcn_ds_read_tr16_b64_v4i16((LAS s16x4*)p0));
  const u32x2 b = __builtin_bit_cast(u32x2, __builtin_amdgcn_ds_read_tr16_b64_v4i16((LAS s16x4*)p1));
  u32x4 r; r[0] = a[0]; r[1] = a[1]; r[2] = b[0]; r[3] = b[1]; return r;
}
DI void glds(unsigned m0v, unsigned voff, rsrc_t rs, unsigned soff) {
  asm volatile("s_mov_b32 m0, %0\n\ts_nop 0\n\tbuffer_load_dwordx4 %1, %2, %3 offen lds"
               :: "s"(m0v), "v"(voff), "s"(rs), "s"(soff) : "m0", "memory");
}
#define GLD16(dst, ptr, off) asm volatile("global_load_dwordx4 %0, %1, off offset:%2" : "=v"(dst) : "v"(ptr), "i"(off) : "memory")
#define VMWAIT(n) asm volatile("s_waitcnt vmcnt(%0)" :: "i"(n) : "memory")
#define LGWAIT() asm volatile("s_waitcnt lgkmcnt(0)" ::: "memory")
#define TIE(v) asm volatile("" : "+v"(v))
DI void bar() { asm volatile("" ::: "memory"); __builtin_amdgcn_s_barrier(); asm volatile("" ::: "memory"); }
DI void st_sc1(u32x4 v, rsrc_t rs, unsigned off) { __builtin_amdgcn_raw_buffer_store_b128(v, rs, off, 0, 16); }
DI u32x4 ld_sc1(rsrc_t rs, unsigned off) { return __builtin_amdgcn_raw_buffer_load_b128(rs, off, 0, 16); }
DI void set_flag(char* ws, int idx, u64 tag) {
  __hip_atomic_store((gu64*)(ws + WS_FLAG + idx * 64), tag, __ATOMIC_RELAXED, __HIP_MEMORY_SCOPE_AGENT);
}
DI void wait_flags(char* ws, int idx, u64 tag) {
  const int ii = idx < 0 ? 0 : idx;
  for (unsigned spins = 0; spins < SPIN_CAP; ++spins) {
    const u64 v = __hip_atomic_load((gu64*)(ws + WS_FLAG + ii * 64), __ATOMIC_RELAXED, __HIP_MEMORY_SCOPE_AGENT);
    if (__all(idx < 0 || v == tag)) break;
    __builtin_amdgcn_s_sleep(1);
  }
  __builtin_amdgcn_fence(__ATOMIC_ACQUIRE, "wavefront");
}

DI void reduce_job(char* ws, rsrc_t wsr, int job, int lane, u64 tag) {
  const int h = job >> 1, u = job & 1;
  wait_flags(ws, lane < 16 ? F_KV + (lane * 8 + (h >> 1)) * 8 + (h & 1) * 4 : -1, tag);
  u32x4 v[16][2];
#pragma unroll
  for (int c = 0; c < 16; ++c)
#pragma unroll
    for (int t = 0; t < 2; ++t) v[c][t] = ld_sc1(wsr, WS_PART + (unsigned)(((c * 16 + h) * 4 + u * 2 + t) * 1024 + lane * 16));
  f32x4 s0 = __builtin_bit_cast(f32x4, v[0][0]), s1 = __builtin_bit_cast(f32x4, v[0][1]);
#pragma unroll
  for (int c = 1; c < 16; ++c) { s0 += __builtin_bit_cast(f32x4, v[c][0]); s1 += __builtin_bit_cast(f32x4, v[c][1]); }
  u32x4 o; o[0] = pk2(s0[0], s0[1]); o[1] = pk2(s0[2], s0[3]); o[2] = pk2(s1[0], s1[1]); o[3] = pk2(s1[2], s1[3]);
  st_sc1(o, wsr, WS_KVF + (unsigned)(job * 1024 + lane * 16));
}

constexpr int A_R = 114688;
constexpr int B_SLOT = 39936, B_OX = 21504, B_OH = 37888;
DI constexpr int a_qbase(int ks) { return A_R + (ks < 9 ? 16384 + ks * 3584 : (ks < 13 ? (ks - 9) * 3584 : 16384)); }

template <bool IS_A>
DI void loader(const char* x, const char* W, const char* wo, char* ws, LAS char* lds, unsigned ldsb, int chunk, int hp, int cg, int w, int lane, int job) {
  const int r0 = chunk * 128;
  u64 tag = 0;
  if (job >= 0) asm volatile("global_load_dwordx2 %0, %1, off sc1" : "=v"(tag) : "v"(ws) : "memory");
  const bool act = lane < 56;
  if (w == 8) {
    const rsrc_t wr = __builtin_amdgcn_make_buffer_rsrc((void*)W, 0, 448 * WROW, 0x00020000);
    const int sl = lane / 7, sg = lane - sl * 7;
    const unsigned vo0 = (unsigned)((4 * (sl & 1) + (sl >> 1)) * WROW + sg * 16), vo1 = vo0 + 8 * WROW;
    const unsigned so = (unsigned)(hp * 112);
    auto stage = [&](int st) {
      unsigned d = ldsb + (IS_A ? A_R + (st % 3) * 14336 : (st & 3) * B_SLOT), s2 = so + st * 64 * WROW;
#pragma unroll 1
      for (int jj = 0; jj < 4; ++jj) {
        if (act) {
          glds(d, vo0, wr, s2 + 896); glds(d + 7168, vo0, wr, s2 + 1792);
          if constexpr (!IS_A) glds(d + 14336, vo0, wr, s2);
          glds(d + 896, vo1, wr, s2 + 896); glds(d + 7168 + 896, vo1, wr, s2 + 1792);
          if constexpr (!IS_A) glds(d + 14336 + 896, vo1, wr, s2);
        }
        d += 1792; s2 += 16 * WROW;
      }
    };
    auto qgroups = [&](unsigned d, int g0, int n) {
      unsigned s2 = so + g0 * 16 * WROW;
#pragma unroll 1
      for (int jj = 0; jj < n; ++jj) { if (act) { glds(d, vo0, wr, s2); glds(d + 896, vo1, wr, s2); } d += 1792; s2 += 16 * WROW; }
    };
    if constexpr (IS_A) {
      stage(0); stage(1); stage(2);
      VMWAIT(32); bar();
#pragma unroll 1
      for (int st = 1; st < 5; ++st) { VMWAIT(16); bar(); stage(st + 2); }
      VMWAIT(16); bar();
      qgroups(ldsb + a_qbase(0), 0, 6);
      VMWAIT(12); bar();
      qgroups(ldsb + a_qbase(3), 6, 12);
      bar(); bar();
      VMWAIT(0); bar();
      bar();
      qgroups(ldsb + a_qbase(9), 18, 8);
      VMWAIT(0); bar();
      qgroups(ldsb + a_qbase(13), 26, 2);
      VMWAIT(0); bar();
      bar();
    } else {
      stage(0); stage(1);
#pragma unroll 1
      for (int st = 0; st < 5; ++st) { VMWAIT(24); bar(); stage(st + 2); }
      VMWAIT(24); bar();
      VMWAIT(0); bar();
      bar();
    }
  } else {
    const rsrc_t xr = __builtin_amdgcn_make_buffer_rsrc((void*)x, 0, 2048 * XROW, 0x00020000);
    const unsigned vo = (unsigned)((lane >> 3) * XROW + (((lane & 7) ^ (lane >> 3)) * 16));
    const int hbase = r0 - 16 < 0 ? 0 : r0 - 16;
    auto stage = [&](int st) {
      unsigned d = ldsb + (IS_A ? st * 16384 : (st & 3) * B_SLOT + B_OX), s2 = (unsigned)(r0 * XROW + st * 128);
#pragma unroll 1
      for (int jj = 0; jj < 4; ++jj) {
        glds(d, vo, xr, s2); glds(d + 1024, vo, xr, s2 + 8 * XROW); glds(d + 2048, vo, xr, s2 + 16 * XROW); glds(d + 3072, vo, xr, s2 + 24 * XROW);
        d += 4096; s2 += 32 * XROW;
      }
      if constexpr (!IS_A) {
        glds(d, vo, xr, (unsigned)(hbase * XROW + st * 128));
        glds(d + 1024, vo, xr, (unsigned)((hbase + 8) * XROW + st * 128));
      }
    };
    constexpr int PER = IS_A ? 16 : 18;
    stage(0); stage(1); stage(2);
#pragma unroll 1
    for (int st = 0; st < 4; ++st) { VMWAIT(2 * PER); bar(); stage(st + 3); }
    VMWAIT(2 * PER); bar();
    VMWAIT(PER); bar();
    VMWAIT(0); bar();
    bar();
    if constexpr (IS_A) { bar(); bar(); bar(); bar(); bar(); bar(); }
  }
  auto wo_image = [&]() {
    const rsrc_t rs = __builtin_amdgcn_make_buffer_rsrc((void*)wo, 0, 448 * XROW, 0x00020000);
    const unsigned row = lane / 14, seg = lane - row * 14;
    const unsigned voff = row * XROW + seg * 16;
    unsigned d = ldsb + L_WO + (w & 1) * 896, s2 = (w & 1) * 4 * XROW + cg * 224;
#pragma unroll 1
    for (int j = 0; j < 14; ++j) {
      if (act) { glds(d, voff, rs, s2); glds(d + 1792, voff, rs, s2 + 8 * XROW); glds(d + 3584, voff, rs, s2 + 16 * XROW); glds(d + 5376, voff, rs, s2 + 24 * XROW); }
      d += 7168; s2 += 32 * XROW;
    }
    if (act && w == 8) { u32x4 z = {0, 0, 0, 0}; *(LAS u32x4*)(lds + L_WO + 448 * 224 + lane * 16) = z; }
  };
  if constexpr (!IS_A) {
    bar();
    if (job >= 0) {
      TIE(tag); tag += 1;
      const rsrc_t wsr = __builtin_amdgcn_make_buffer_rsrc((void*)ws, 0, WS_BYTES, 0x00020000);
      reduce_job(ws, wsr, job, lane, tag);
      VMWAIT(0);
      if (lane == 0) set_flag(ws, F_RED + job, tag);
    }
  }
  if (w == 8) wo_image();
  bar();
  if (w == 9) wo_image();
  VMWAIT(0); LGWAIT(); bar();
  bar(); bar();
}

template <bool IS_A>
DI void phase1(char* ws, rsrc_t wsr, LAS char* lds, int chunk, int hp, int w, int lane, u64& tag, int job) {
  const int mg = w & 3, hh = w >> 2, g = lane >> 4, i = lane & 15;
  const int head = 2 * hp + hh;
  const int q = (lane >> 2) & 3, p = lane & 3;
  int toff[2][2];
#pragma unroll
  for (int t = 0; t < 2; ++t)
#pragma unroll
    for (int rd = 0; rd < 2; ++rd)
      toff[t][rd] = (16 * (g >> 1) + 2 * (4 * (g & 1) + q) + rd) * 112 + (28 * hh + 16 * t + 4 * p) * 2;
  int xoff[2];
#pragma unroll
  for (int kk = 0; kk < 2; ++kk) xoff[kk] = (32 * mg + i) * 128 + (((4 * kk + g) ^ (i & 7)) * 16);
  const f32x4 z4 = {0.f, 0.f, 0.f, 0.f};
#define WFR(base, t) trf(lds + (base) + toff[t][0], lds + (base) + toff[t][1])

  if constexpr (IS_A) {
#define KB(ks) (A_R + (((ks) >> 1) % 3) * 14336 + ((ks) & 1) * 3584)
#define XFR(ks, m) (*(const LAS u32x4*)(lds + ((ks) >> 1) * 16384 + xoff[(ks) & 1] + (m) * 2048))
    f32x4 ak[2][2] = {{z4, z4}, {z4, z4}}, av[2][2] = {{z4, z4}, {z4, z4}};
    u32x4 wk[2][2], wv[2][2], xf[2][2];
    bar();

#pragma unroll
    for (int t = 0; t < 2; ++t) { wk[0][t] = WFR(KB(0), t); wv[0][t] = WFR(KB(0) + 7168, t); xf[0][t] = XFR(0, t); }
#pragma unroll
    for (int ks = 0; ks < 14; ++ks) {
      if (ks + 1 < 14) {
        if (((ks + 1) & 1) == 0) { LGWAIT(); bar(); }
#pragma unroll
        for (int t = 0; t < 2; ++t) {
          wk[(ks + 1) & 1][t] = WFR(KB(ks + 1), t); wv[(ks + 1) & 1][t] = WFR(KB(ks + 1) + 7168, t); xf[(ks + 1) & 1][t] = XFR(ks + 1, t);
        }
      }
#pragma unroll
      for (int t = 0; t < 2; ++t)
#pragma unroll
        for (int m = 0; m < 2; ++m) {
          ak[t][m] = mm(xf[ks & 1][m], wk[ks & 1][t], ak[t][m]);
          av[t][m] = mm(xf[ks & 1][m], wv[ks & 1][t], av[t][m]);
        }
    }
    u32x4 pkf[2], vf[2];
#pragma unroll
    for (int t = 0; t < 2; ++t) {
      const unsigned keep = ((t == 1) && (i >= 12)) ? 0u : ~0u;
      const unsigned one = ((t == 1) && (i == 12)) ? 0x3f803f80u : 0u;
#pragma unroll
      for (int m = 0; m < 2; ++m) {
        pkf[t][2 * m] = phi2(ak[t][m][0], ak[t][m][1]) & keep; pkf[t][2 * m + 1] = phi2(ak[t][m][2], ak[t][m][3]) & keep;
        vf[t][2 * m] = (pk2(av[t][m][0], av[t][m][1]) & keep) | one; vf[t][2 * m + 1] = (pk2(av[t][m][2], av[t][m][3]) & keep) | one;
      }
    }
    f32x4 kvp[2][2];
#pragma unroll
    for (int u = 0; u < 2; ++u)
#pragma unroll
      for (int t = 0; t < 2; ++t) kvp[u][t] = mm(pkf[t], vf[u], z4);
    VMWAIT(0); TIE(tag); tag += 1;
    bar();
    LAS char* xs = lds + A_R + (hh * 2 + (mg >> 1)) * 4096 + lane * 16;
    if (mg & 1) {
#pragma unroll
      for (int j = 0; j < 4; ++j) *(LAS f32x4*)(xs + j * 1024) = kvp[j >> 1][j & 1];
    }
    LGWAIT(); bar();
    if (!(mg & 1)) {
#pragma unroll
      for (int j = 0; j < 4; ++j) kvp[j >> 1][j & 1] += *(const LAS f32x4*)(xs + j * 1024);
      if (mg == 2) {
#pragma unroll
        for (int j = 0; j < 4; ++j) *(LAS f32x4*)(xs + j * 1024) = kvp[j >> 1][j & 1];
      }
    }
    LGWAIT(); bar();
    if (mg == 0) {
#pragma unroll
      for (int j = 0; j < 4; ++j) {
        const f32x4 s = kvp[j >> 1][j & 1] + *(const LAS f32x4*)(xs + 4096 + j * 1024);
        st_sc1(__builtin_bit_cast(u32x4, s), wsr, WS_PART + (unsigned)(((chunk * 16 + head) * 4 + j) * 1024 + lane * 16));
      }
    }
    f32x4 aq[2][2] = {{z4, z4}, {z4, z4}};
    u32x4 wq[2][2];
#pragma unroll
    for (int t = 0; t < 2; ++t) { wq[0][t] = WFR(a_qbase(0), t); xf[0][t] = XFR(0, t); }
#pragma unroll
    for (int ks = 0; ks < 14; ++ks) {
      if (ks + 1 < 14) {
        if (ks + 1 == 3 || ks + 1 == 9 || ks + 1 == 13) { LGWAIT(); bar(); }
        if (ks + 1 == 7) {
          VMWAIT(0);
          if (mg == 0 && lane == 0) set_flag(ws, F_KV + (chunk * 8 + hp) * 8 + w, tag);
        }
#pragma unroll
        for (int t = 0; t < 2; ++t) { wq[(ks + 1) & 1][t] = WFR(a_qbase(ks + 1), t); xf[(ks + 1) & 1][t] = XFR(ks + 1, t); }
      }
#pragma unroll
      for (int t = 0; t < 2; ++t)
#pragma unroll
        for (int m = 0; m < 2; ++m) aq[t][m] = mm(wq[ks & 1][t], xf[ks & 1][m], aq[t][m]);
    }
    bar();
    if (job >= 0) reduce_job(ws, wsr, job, lane, tag);
#pragma unroll
    for (int m = 0; m < 2; ++m) {
      u32x4 o;
#pragma unroll
      for (int t = 0; t < 2; ++t) {
        const unsigned keep = ((t == 1) && (g == 3)) ? 0u : ~0u;
        o[2 * t] = phi2(aq[t][m][0], aq[t][m][1]) & keep; o[2 * t + 1] = phi2(aq[t][m][2], aq[t][m][3]) & keep;
      }
      st_sc1(o, wsr, WS_PQ + (unsigned)(((chunk * 8 + 2 * mg + m) * 16 + head) * 1024 + lane * 16));
    }
    VMWAIT(0);
    if (lane == 0) { set_flag(ws, F_PQ + (chunk * 8 + hp) * 8 + w, tag); if (job >= 0) set_flag(ws, F_RED + job, tag); }
    bar();
  } else {
#define SB(ks) ((((ks) >> 1) & 3) * B_SLOT)
#define XFB(ks, m) (*(const LAS u32x4*)(lds + SB(ks) + B_OX + xoff[(ks) & 1] + (m) * 2048))
    f32x4 aq[2][2] = {{z4, z4}, {z4, z4}}, ak[2][2] = {{z4, z4}, {z4, z4}}, av[2][2] = {{z4, z4}, {z4, z4}}, ah = z4;
    u32x4 wq[2][2], wk[2][2], wv[2][2], xf[2][2], hx[2], hw[2];
    const int hw0 = ((mg & 2) ? 7168 : 0) + ((mg & 1) ? toff[1][0] : toff[0][0]);
    const int hw1 = ((mg & 2) ? 7168 : 0) + ((mg & 1) ? toff[1][1] : toff[0][1]);
#define HWFR(ks) trf(lds + SB(ks) + ((ks) & 1) * 3584 + hw0, lds + SB(ks) + ((ks) & 1) * 3584 + hw1)
    int hoff[2];
#pragma unroll
    for (int kk = 0; kk < 2; ++kk) hoff[kk] = B_OH + i * 128 + (((4 * kk + g) ^ (i & 7)) * 16);
#define HFR(ks) (*(const LAS u32x4*)(lds + SB(ks) + hoff[(ks) & 1]))
    bar();
#pragma unroll
    for (int t = 0; t < 2; ++t) { wq[0][t] = WFR(SB(0) + 14336, t); wk[0][t] = WFR(SB(0), t); wv[0][t] = WFR(SB(0) + 7168, t); xf[0][t] = XFB(0, t); }
    hx[0] = HFR(0); hw[0] = HWFR(0);
#pragma unroll
    for (int ks = 0; ks < 14; ++ks) {
      if (ks + 1 < 14) {
        if (((ks + 1) & 1) == 0) { LGWAIT(); bar(); }
        const int bb = SB(ks + 1) + ((ks + 1) & 1) * 3584;
#pragma unroll
        for (int t = 0; t < 2; ++t) {
          wq[(ks + 1) & 1][t] = WFR(bb + 14336, t); wk[(ks + 1) & 1][t] = WFR(bb, t); wv[(ks + 1) & 1][t] = WFR(bb + 7168, t);
          xf[(ks + 1) & 1][t] = XFB(ks + 1, t);
        }
        hx[(ks + 1) & 1] = HFR(ks + 1); hw[(ks + 1) & 1] = HWFR(ks + 1);
      }
#pragma unroll
      for (int t = 0; t < 2; ++t)
#pragma unroll
        for (int m = 0; m < 2; ++m) {
          aq[t][m] = mm(wq[ks & 1][t], xf[ks & 1][m], aq[t][m]);
          ak[t][m] = mm(wk[ks & 1][t], xf[ks & 1][m], ak[t][m]);
          av[t][m] = mm(xf[ks & 1][m], wv[ks & 1][t], av[t][m]);
        }
      ah = mm(hw[ks & 1], hx[ks & 1], ah);
    }
    u32x4 qf[2], kf[2];
    u32x2 vfr[2][2];
#pragma unroll
    for (int m = 0; m < 2; ++m) {
#pragma unroll
      for (int t = 0; t < 2; ++t) {
        const unsigned keep = ((t == 1) && (g == 3)) ? 0u : ~0u;
        qf[m][2 * t] = pk2(aq[t][m][0], aq[t][m][1]) & keep; qf[m][2 * t + 1] = pk2(aq[t][m][2], aq[t][m][3]) & keep;
        kf[m][2 * t] = pk2(ak[t][m][0], ak[t][m][1]) & keep; kf[m][2 * t + 1] = pk2(ak[t][m][2], ak[t][m][3]) & keep;
        vfr[t][m][0] = pk2(av[t][m][0], av[t][m][1]); vfr[t][m][1] = pk2(av[t][m][2], av[t][m][3]);
      }
    }
    VMWAIT(0); TIE(tag); tag += 1;
    bar();
    {
      if (mg < 3) {
        LAS char* e = lds + (hh * 4 + mg + 1) * 2048;
        *(LAS u32x4*)(e + lane * 16) = kf[1];
        *(LAS u32x2*)(e + 1024 + lane * 8) = vfr[0][1];
        *(LAS u32x2*)(e + 1536 + lane * 8) = vfr[1][1];
      }
      LAS char* e0 = lds + (hh * 4) * 2048;
      const unsigned keep = ((chunk == 0) || ((mg == 1) && (g == 3))) ? 0u : ~0u;
      u32x2 hv; hv[0] = pk2(ah[0], ah[1]) & keep; hv[1] = pk2(ah[2], ah[3]) & keep;
      if (mg & 2) {
        LAS unsigned short* vt = (LAS unsigned short*)(e0 + 1024 + (mg & 1) * 512) + ((i >> 2) * 16 + 4 * g) * 4 + (i & 3);
        vt[0] = (unsigned short)hv[0]; vt[4] = (unsigned short)(hv[0] >> 16); vt[8] = (unsigned short)hv[1]; vt[12] = (unsigned short)(hv[1] >> 16);
      } else *(LAS u32x2*)(e0 + lane * 16 + (mg & 1) * 8) = hv;
    }
    LGWAIT(); bar();
    u32x4 kprev; u32x2 vprev[2];
    {
      const LAS char* e = lds + (hh * 4 + mg) * 2048;
      kprev = *(const LAS u32x4*)(e + lane * 16);
      vprev[0] = *(const LAS u32x2*)(e + 1024 + lane * 8);
      vprev[1] = *(const LAS u32x2*)(e + 1536 + lane * 8);
    }
    const float scale = 0.18898223650461362f;
#pragma unroll
    for (int m = 0; m < 2; ++m) {
      const u32x4 kp = m ? kf[0] : kprev;
      const f32x4 sp = mm(kp, qf[m], z4), so = mm(kf[m], qf[m], z4);
      const bool first = (chunk == 0) && (mg == 0) && (m == 0);
      float l[8]; bool ok[8]; float mx = -3.0e38f;
#pragma unroll
      for (int r = 0; r < 4; ++r) {
        const int dp = i + 16 - (4 * g + r), dn = i - (4 * g + r);
        ok[r] = (dp <= 14) && !(dp & 1) && !first;
        ok[4 + r] = (dn >= 0) && (dn <= 14) && !(dn & 1);
        l[r] = bfr(bfr(sp[r]) * scale); l[4 + r] = bfr(bfr(so[r]) * scale);
        mx = fmaxf(mx, ok[r] ? l[r] : -3.0e38f); mx = fmaxf(mx, ok[4 + r] ? l[4 + r] : -3.0e38f);
      }
      mx = fmaxf(mx, __shfl_xor(mx, 16)); mx = fmaxf(mx, __shfl_xor(mx, 32));
      float sum = 0.f;
#pragma unroll
      for (int r = 0; r < 8; ++r) { l[r] = ok[r] ? __builtin_amdgcn_exp2f((l[r] - mx) * 1.4426950408889634f) : 0.f; sum += l[r]; }
      sum += __shfl_xor(sum, 16); sum += __shfl_xor(sum, 32);
      const float inv = 1.f / sum;
      u32x4 pf;
#pragma unroll
      for (int r = 0; r < 4; ++r) pf[r] = pk2(l[2 * r] * inv, l[2 * r + 1] * inv);
      u32x4 o;
#pragma unroll
      for (int u = 0; u < 2; ++u) {
        const u32x2 va = m ? vfr[u][0] : vprev[u], vb = vfr[u][m];
        u32x4 vv; vv[0] = va[0]; vv[1] = va[1]; vv[2] = vb[0]; vv[3] = vb[1];
        const f32x4 oo = mm(vv, pf, z4);
        const unsigned keep = ((u == 1) && (g == 3)) ? 0u : ~0u;
        o[2 * u] = pk2(oo[0], oo[1]) & keep; o[2 * u + 1] = pk2(oo[2], oo[3]) & keep;
      }
      st_sc1(o, wsr, WS_OB + (unsigned)(((chunk * 8 + 2 * mg + m) * 16 + head) * 1024 + lane * 16));
    }
    VMWAIT(0);
    if (lane == 0) set_flag(ws, F_OB + (chunk * 8 + hp) * 8 + w, tag);
    bar();
  }
}

template <bool IS_A>
DI void phase2(char* out, char* ws, rsrc_t wsr, LAS char* lds, int tt, int cg, int w, int lane, u64 tag, bool updater) {
  const int g = lane >> 4, i = lane & 15, q = (lane >> 2) & 3, p = lane & 3;
  const int tile = tt * 4 + (w & 3), kh = w >> 2;
  const f32x4 z4 = {0.f, 0.f, 0.f, 0.f};
  {
    const int hd = 8 * kh + (lane & 7);
    int fi = lane < 8 ? (IS_A ? F_PQ : F_OB) + ((tile >> 3) * 8 + (hd >> 1)) * 8 + (hd & 1) * 4 + ((tile & 7) >> 1) : -1;
    if (IS_A && lane >= 8 && lane < 12) fi = F_RED + 4 * w + lane - 8;
    wait_flags(ws, fi, tag);
  }
  u32x4 of[8];
  if constexpr (IS_A) {
    u32x4 t4[4];
#pragma unroll
    for (int j = 0; j < 4; ++j) t4[j] = __builtin_amdgcn_raw_buffer_load_b128(wsr, WS_KVF + (unsigned)((4 * w + j) * 1024 + lane * 16), 0, KVAUX);
#pragma unroll
    for (int h = 0; h < 8; ++h) of[h] = ld_sc1(wsr, WS_PQ + (unsigned)((tile * 16 + 8 * kh + h) * 1024 + lane * 16));
#pragma unroll
    for (int j = 0; j < 4; ++j) *(LAS u32x4*)(lds + (4 * w + j) * 1024 + lane * 16) = t4[j];
  } else {
#pragma unroll
    for (int h = 0; h < 8; ++h) of[h] = ld_sc1(wsr, WS_OB + (unsigned)((tile * 16 + 8 * kh + h) * 1024 + lane * 16));
  }
  LGWAIT(); bar();
  const LAS char* b0 = lds + L_WO + (8 * kh * 28 + 4 * g + q) * 224 + 8 * p;
  u32x4 wf[2][7];
#pragma unroll
  for (int n = 0; n < 7; ++n) wf[0][n] = trf(b0 + n * 32, b0 + 16 * 224 + n * 32);
  if constexpr (IS_A) {
    f32x4 n0[8], n1[8];
    float den[8];
#pragma unroll
    for (int h = 0; h < 8; ++h) {
      const u32x4 k0 = *(const LAS u32x4*)(lds + ((8 * kh + h) * 2) * 1024 + lane * 16);
      const u32x4 k1 = *(const LAS u32x4*)(lds + ((8 * kh + h) * 2 + 1) * 1024 + lane * 16);
      n0[h] = mm(k0, of[h], z4); n1[h] = mm(k1, of[h], z4);
    }
#pragma unroll
    for (int h = 0; h < 8; ++h) den[h] = __shfl(n1[h][0], 48 + i);
    const unsigned keep = (g == 3) ? 0u : ~0u;
#pragma unroll
    for (int h = 0; h < 8; ++h) {
      const float inv = __builtin_amdgcn_rcpf(bfr(bfr(den[h]) + 1e-6f));
      u32x4 o;
      o[0] = rmul2(n0[h][0], n0[h][1], inv); o[1] = rmul2(n0[h][2], n0[h][3], inv);
      o[2] = rmul2(n1[h][0], n1[h][1], inv) & keep; o[3] = rmul2(n1[h][2], n1[h][3], inv) & keep;
      of[h] = o;
    }
  }
  f32x4 acc[7];
#pragma unroll
  for (int n = 0; n < 7; ++n) acc[n] = z4;
#pragma unroll
  for (int h = 0; h < 8; ++h) {
    if (h + 1 < 8) {
#pragma unroll
      for (int n = 0; n < 7; ++n) wf[(h + 1) & 1][n] = trf(b0 + (h + 1) * 28 * 224 + n * 32, b0 + ((h + 1) * 28 + 16) * 224 + n * 32);
    }
#pragma unroll
    for (int n = 0; n < 7; ++n) acc[n] = mm(wf[h & 1][n], of[h], acc[n]);
    __builtin_amdgcn_sched_barrier(0);
  }
  LAS char* ex = lds + 32768 + (w & 3) * 7168 + lane * 16;
  if (kh == 0) {
#pragma unroll
    for (int n = 4; n < 7; ++n) *(LAS f32x4*)(ex + n * 1024) = acc[n];
  } else {
#pragma unroll
    for (int n = 0; n < 4; ++n) *(LAS f32x4*)(ex + n * 1024) = acc[n];
  }
  LGWAIT(); bar();
  LAS char* stg = lds + ((w & 3) * 16 + i) * 240 + g * 8;
  if (kh == 0) {
#pragma unroll
    for (int n = 0; n < 4; ++n) {
      const f32x4 v = acc[n] + *(const LAS f32x4*)(ex + n * 1024);
      u32x2 o; o[0] = pk2(v[0], v[1]); o[1] = pk2(v[2], v[3]);
      *(LAS u32x2*)(stg + n * 32) = o;
    }
  } else {
#pragma unroll
    for (int n = 4; n < 7; ++n) {
      const f32x4 v = *(const LAS f32x4*)(ex + n * 1024) + acc[n];
      u32x2 o; o[0] = pk2(v[0], v[1]); o[1] = pk2(v[2], v[3]);
      *(LAS u32x2*)(stg + n * 32) = o;
    }
  }
  LGWAIT(); bar();
  if (lane < 56) {
    const int row = lane / 14, seg = lane - row * 14;
#pragma unroll
    for (int j = 0; j < 2; ++j) {
      const int r = (w * 2 + j) * 4 + row;
      const u32x4 v = *(const LAS u32x4*)(lds + r * 240 + seg * 16);
      *(u32x4*)(out + (size_t)(tt * 64 + r) * 1792 + (IS_A ? 0 : 896) + cg * 224 + seg * 16) = v;
    }
  }
  if (updater && w == 0) {
    wait_flags(ws, lane < 32 ? F_RED + lane : -1, tag);
    wait_flags(ws, F_OB + lane * 8, tag);
    wait_flags(ws, F_OB + (64 + lane) * 8, tag);
    if (lane == 0) __hip_atomic_store((gu64*)ws, tag, __ATOMIC_RELAXED, __HIP_MEMORY_SCOPE_AGENT);
  }
}

__global__ void __launch_bounds__(640, 3)
fused_kernel(const char* xA, const char* wA, const char* woA, const char* xB, const char* wB, const char* woB,
             char* out, char* ws) {
  __shared__ __attribute__((aligned(16))) char smem[LDS_BYTES];
  LAS char* lds = (LAS char*)smem;
  const unsigned ldsb = (unsigned)(uintptr_t)lds;
  const int lane = threadIdx.x & 63;
  const int w = __builtin_amdgcn_readfirstlane(threadIdx.x >> 6);
  const int b = blockIdx.x, xcd = b & 7, idx = b >> 3, xq = xcd >> 1;
  const bool isA = (xcd & 1) == 0;
  const int chunk = (xq & 1) * 8 + (idx & 7), hp = (xq >> 1) * 4 + (idx >> 3);
  const int tt = xq * 8 + (idx & 7), cg = idx >> 3;
  if (w >= 8) {
    const int ub = xq * 32 + idx;
    if (isA) loader<true>(xA, wA, woA, ws, lds, ldsb, chunk, hp, cg, w, lane, -1);
    else     loader<false>(xB, wB, woB, ws, lds, ldsb, chunk, hp, cg, w, lane, ((ub & 3) == 2 && w == 9) ? (ub >> 2) : -1);
    return;
  }
  const int job = -1;
  const rsrc_t wsr = __builtin_amdgcn_make_buffer_rsrc((void*)ws, 0, WS_BYTES, 0x00020000);
  u64 tag;
  asm volatile("global_load_dwordx2 %0, %1, off sc1" : "=v"(tag) : "v"(ws) : "memory");
  if (isA) {
    phase1<true>(ws, wsr, lds, chunk, hp, w, lane, tag, job);
    phase2<true>(out, ws, wsr, lds, tt, cg, w, lane, tag, false);
  } else {
    phase1<false>(ws, wsr, lds, chunk, hp, w, lane, tag, -1);
    phase2<false>(out, ws, wsr, lds, tt, cg, w, lane, tag, b == 1);
  }
}

extern "C" void kernel_launch(void* const* d_in, const int* in_sizes, int n_in,
                              void* d_out, int out_size, void* d_ws, size_t ws_size,
                              hipStream_t stream) {
  fused_kernel<<<256, 640, 0, stream>>>((const char*)d_in[0], (const char*)d_in[1], (const char*)d_in[2],
                                        (const char*)d_in[3], (const char*)d_in[4], (const char*)d_in[5],
                                        (char*)d_out, (char*)d_ws);
}
```
